```python
import torch

def _softmax(x, axis=-1):
    return torch.softmax(x, dim=axis)
def _gelu(x):
    return torch.nn.functional.gelu(x, approximate="tanh")
def _silu(x):
    return torch.nn.functional.silu(x)
def _sigmoid(x):
    return torch.sigmoid(x)
def _layernorm(x, axis=-1, eps=1e-5):
    m = x.mean(dim=axis, keepdim=True)
    v = x.var(dim=axis, keepdim=True, unbiased=False)
    return (x - m) / torch.sqrt(v + eps)
def _scatter_add(tgt, idx, src, axis=0):
    return tgt.clone().index_add_(axis, idx, src)
def _segment_sum(src, seg, n):
    out = torch.zeros((n,) + src.shape[1:], dtype=src.dtype)
    return out.index_add_(0, seg, src)
def _pad(x, pw):
    flat = tuple(v for pair in reversed(pw) for v in pair)
    return torch.nn.functional.pad(x, flat)

kernel_name = 'sequential_preLN__grouped_kv_mqa__glu_gated_swiglu__prompt'

def setup_inputs():

    _w = (lambda a: (lambda t: t.to(torch.bfloat16) if t.is_floating_point() else t)(torch.as_tensor(a)))
    def _A_setup():
        B, S, D, H = 1, 2048, 512, 32
        Dh = D // H
        return {
            "x": _w((lambda *s: torch.randn(*s, dtype=torch.bfloat16) * 0.05)(B, S, D)),
            "wq": _w((lambda *s: torch.randn(*s, dtype=torch.bfloat16) * 0.05)(D, H * Dh)),
            "wk": _w((lambda *s: torch.randn(*s, dtype=torch.bfloat16) * 0.05)(D, Dh)),
            "wv": _w((lambda *s: torch.randn(*s, dtype=torch.bfloat16) * 0.05)(D, Dh)),
            "wo": _w((lambda *s: torch.randn(*s, dtype=torch.bfloat16) * 0.05)(H * Dh, D)),
        }
    _A_in = _A_setup()

    def _B_setup():
        B, S, D, F = 1, 2048, 512, 1376
        return {
            "x": _w((lambda *s: torch.randn(*s, dtype=torch.bfloat16) * 0.05)(B, S, D)),
            "w1": _w((lambda *s: torch.randn(*s, dtype=torch.bfloat16) * 0.4)(D, F) * (2.0 / D) ** 0.5),
            "w3": _w((lambda *s: torch.randn(*s, dtype=torch.bfloat16) * 0.4)(D, F) * (2.0 / D) ** 0.5),
            "w2": _w((lambda *s: torch.randn(*s, dtype=torch.bfloat16) * 0.4)(F, D) * (2.0 / F) ** 0.5),
        }
    _B_in = _B_setup()

    return {
        "A_x": _A_in["x"],
        "A_wq": _A_in["wq"],
        "A_wk": _A_in["wk"],
        "A_wv": _A_in["wv"],
        "A_wo": _A_in["wo"],
        "B_x": _B_in["x"],
        "B_w1": _B_in["w1"],
        "B_w3": _B_in["w3"],
        "B_w2": _B_in["w2"],
    }

def reference(A_x, A_wq, A_wk, A_wv, A_wo, B_x, B_w1, B_w3, B_w2):
    def _A_ref(x, wq, wk, wv, wo):
        B, S, D = x.shape
        H = 32
        Dh = D // H
        q = (lambda x, *d: x.permute(*d))((x @ wq).reshape(B, S, H, Dh), 0, 2, 1, 3)
        k = x @ wk
        v = x @ wv
        scores = torch.einsum("bhqd,bkd->bhqk", q, k) * (Dh ** -0.5)
        p = _softmax(scores, axis=-1)
        o = torch.einsum("bhqk,bkd->bhqd", p, v)
        return (lambda x, *d: x.permute(*d))(o, 0, 2, 1, 3).reshape(B, S, H * Dh) @ wo

    def _B_ref(x, w1, w3, w2):
        gate = torch.einsum("bsd,df->bsf", x, w1)
        up = torch.einsum("bsd,df->bsf", x, w3)
        h = _silu(gate) * up
        out = torch.einsum("bsf,fd->bsd", h, w2)
        return out

    x = A_x
    h = x + _A_ref(_layernorm(x, axis=-1), A_wq, A_wk, A_wv, A_wo)
    h2 = h + _B_ref(_layernorm(h, axis=-1), B_w1, B_w3, B_w2)
    return h2
```

```cpp
#include <hip/hip_runtime.h>
#include <hip/hip_bf16.h>

typedef __attribute__((ext_vector_type(8))) short bf16x8;
typedef __attribute__((ext_vector_type(4))) short s16x4;
typedef __attribute__((ext_vector_type(16))) float f32x16;
typedef __attribute__((ext_vector_type(4))) float f32x4;
typedef __attribute__((ext_vector_type(4))) unsigned u32x4;
typedef __attribute__((ext_vector_type(2))) unsigned u32x2;
typedef __attribute__((address_space(3))) s16x4 lds_s16x4;

#define DI __device__ __forceinline__

constexpr int S_LEN = 2048, DM = 512, NH = 32, DH = 16, FF = 1376;
constexpr float QSCALE = 0.25f * 1.4426950408889634f;

constexpr size_t WS_Q = 0;
constexpr size_t WS_KF = WS_Q + (size_t)S_LEN * DM * 2;
constexpr size_t WS_VF = WS_KF + (size_t)S_LEN * DH * 2;
constexpr size_t WS_O = WS_VF + (size_t)S_LEN * DH * 2;
constexpr size_t WS_H = WS_O + (size_t)S_LEN * DM * 2;
constexpr size_t WS_G = WS_H + (size_t)S_LEN * DM * 2;

DI unsigned cvtpk(float lo, float hi) {
  unsigned r;
  asm("v_cvt_pk_bf16_f32 %0, %1, %2" : "=v"(r) : "v"(lo), "v"(hi));
  return r;
}
DI float bf2f(short v) { return __uint_as_float(((unsigned)(unsigned short)v) << 16); }
DI s16x4 tr_read(const char* p) {
  return __builtin_amdgcn_ds_read_tr16_b64_v4i16((lds_s16x4*)p);
}
DI bf16x8 cat8(s16x4 a, s16x4 b) { return __builtin_shufflevector(a, b, 0, 1, 2, 3, 4, 5, 6, 7); }
DI float wave_sum(float v) {
#pragma unroll
  for (int o = 32; o > 0; o >>= 1) v += __shfl_xor(v, o);
  return v;
}


__global__ __launch_bounds__(256) void k1_ln_qkv(const short* __restrict__ x, const short* __restrict__ wq,
                                                 const short* __restrict__ wk, const short* __restrict__ wv,
                                                 char* __restrict__ ws) {
  extern __shared__ __attribute__((aligned(16))) char smem[];
  char* sA = smem;
  char* sB = smem + 65536;
  char* sC = smem + 131072;
  const int tid = threadIdx.x, lane = tid & 63, w = tid >> 6;
  const int b = blockIdx.x, xcd = b & 7, idx = b >> 3;
  const int mt = xcd * 4 + (idx & 3), nt = idx >> 2;
  const int m0 = mt * 64, n0 = nt * 64;

  bf16x8 xv[16];
#pragma unroll
  for (int i = 0; i < 16; ++i) xv[i] = *(const bf16x8*)(x + (size_t)(m0 + w * 16 + i) * DM + lane * 8);
  bf16x8 bv[16];
#pragma unroll
  for (int i = 0; i < 16; ++i) {
    const int k = i * 32 + (tid >> 3), c = tid & 7;
    bv[i] = *(const bf16x8*)(wq + (size_t)k * DM + n0 + c * 8);
  }
  const short* wkv = (nt & 1) ? wv : wk;
  bf16x8 cv[4];
#pragma unroll
  for (int i = 0; i < 4; ++i) cv[i] = *(const bf16x8*)(wkv + (size_t)(i * 256 + tid) * 8);

#pragma unroll
  for (int i = 0; i < 16; ++i) {
    float f[8];
    float s = 0.f;
#pragma unroll
    for (int j = 0; j < 8; ++j) { f[j] = bf2f(xv[i][j]); s += f[j]; }
    const float mean = wave_sum(s) * (1.f / DM);
    float q = 0.f;
#pragma unroll
    for (int j = 0; j < 8; ++j) { f[j] -= mean; q += f[j] * f[j]; }
    const float rstd = rsqrtf(wave_sum(q) * (1.f / DM) + 1e-5f);
    u32x4 o;
#pragma unroll
    for (int j = 0; j < 4; ++j) o[j] = cvtpk(f[2 * j] * rstd, f[2 * j + 1] * rstd);
    const int row = w * 16 + i;
    *(u32x4*)(sA + row * 1024 + ((lane ^ (row & 15)) << 4)) = o;
  }
#pragma unroll
  for (int i = 0; i < 16; ++i) {
    const int k = i * 32 + (tid >> 3), c = tid & 7;
    *(bf16x8*)(sB + k * 128 + ((c << 4) ^ (((k >> 1) & 1) << 6))) = bv[i];
  }
#pragma unroll
  for (int i = 0; i < 4; ++i) *(bf16x8*)(sC + (i * 256 + tid) * 16) = cv[i];
  __syncthreads();

  const int r32 = lane & 31, h = lane >> 5, g = lane >> 4, q4 = (lane & 15) >> 2, p4 = lane & 3;
  const int wm = w >> 1, wn = w & 1;
  f32x16 acc = {};
  {
    const int arow = wm * 32 + r32;
    const char* ap = sA + arow * 1024;
    const int nb = (wn * 32 + (g & 1) * 16 + 4 * p4) * 2;
#pragma unroll 8
    for (int s = 0; s < 32; ++s) {
      const bf16x8 af = *(const bf16x8*)(ap + (((2 * s + h) ^ (arow & 15)) << 4));
      const int k0 = 16 * s + 8 * h + q4;
      const s16x4 b0 = tr_read(sB + k0 * 128 + (nb ^ (((k0 >> 1) & 1) << 6)));
      const s16x4 b1 = tr_read(sB + (k0 + 4) * 128 + (nb ^ ((((k0 + 4) >> 1) & 1) << 6)));
      acc = __builtin_amdgcn_mfma_f32_32x32x16_bf16(cat8(b0, b1), af, acc, 0, 0, 0);
    }
  }
  {
    short* qo = (short*)(ws + WS_Q) + (size_t)(m0 + wm * 32 + r32) * DM + n0 + wn * 32 + 4 * h;
#pragma unroll
    for (int gg = 0; gg < 4; ++gg) {
      u32x2 o;
      o[0] = cvtpk(acc[4 * gg] * QSCALE, acc[4 * gg + 1] * QSCALE);
      o[1] = cvtpk(acc[4 * gg + 2] * QSCALE, acc[4 * gg + 3] * QSCALE);
      *(u32x2*)(qo + 8 * gg) = o;
    }
  }
  if (w == 0) {
    const int rb = (nt >> 1) * 16;
    const int arow = rb + (lane & 15);
    f32x4 c4 = {};
#pragma unroll
    for (int s = 0; s < 16; ++s) {
      const bf16x8 af = *(const bf16x8*)(sA + arow * 1024 + (((4 * s + g) ^ (arow & 15)) << 4));
      const int k0 = 32 * s + 8 * g + q4;
      const s16x4 b0 = tr_read(sC + k0 * 32 + 8 * p4);
      const s16x4 b1 = tr_read(sC + (k0 + 4) * 32 + 8 * p4);
      const bf16x8 bfr = cat8(b0, b1);
      if (nt & 1) c4 = __builtin_amdgcn_mfma_f32_16x16x32_bf16(af, bfr, c4, 0, 0, 0);
      else        c4 = __builtin_amdgcn_mfma_f32_16x16x32_bf16(bfr, af, c4, 0, 0, 0);
    }
    u32x2 o;
    o[0] = cvtpk(c4[0], c4[1]);
    o[1] = cvtpk(c4[2], c4[3]);
    if (nt & 1) {
      const int key0 = m0 + rb;
      const int t = key0 >> 5, s = (key0 >> 4) & 1;
      short* vf = (short*)(ws + WS_VF) + (size_t)t * 512 + s * 256 + (g & 1) * 128 + (lane & 15) * 8 + (g >> 1) * 4;
      *(u32x2*)vf = o;
    } else {
      const int key = m0 + rb + (lane & 15);
      const int t = key >> 5;
      short* kf = (short*)(ws + WS_KF) + (size_t)t * 512 + (g >> 1) * 256 + (key & 31) * 8 + (g & 1) * 4;
      *(u32x2*)kf = o;
    }
  }
}

constexpr float RESCALE_THR = 8.f;

__global__ __launch_bounds__(512) void k2_attn(char* __restrict__ ws) {
  extern __shared__ __attribute__((aligned(16))) char smem[];
  const int tid = threadIdx.x, lane = tid & 63, w = tid >> 6;
  const int r32 = lane & 31, h = lane >> 5;
  {
    const bf16x8* src = (const bf16x8*)(ws + WS_KF);
    bf16x8 v[16];
#pragma unroll
    for (int i = 0; i < 16; ++i) v[i] = src[i * 512 + tid];
#pragma unroll
    for (int i = 0; i < 16; ++i) *(bf16x8*)(smem + (i * 512 + tid) * 16) = v[i];
  }
  const size_t row = (size_t)blockIdx.x * 256 + w * 32 + r32;
  const bf16x8 qf = *(const bf16x8*)((const short*)(ws + WS_Q) + row * 16 + h * 8);
  __syncthreads();

  const char* kp = smem + lane * 16;
  const char* vp = smem + 65536 + h * 256 + (r32 & 15) * 16;
  const bool vlane = r32 < 16;
  bf16x8 vf0, vf1;
  {
    const short one = (r32 == 16) ? (short)0x3F80 : (short)0;
#pragma unroll
    for (int j = 0; j < 8; ++j) { vf0[j] = one; vf1[j] = one; }
  }
  f32x16 o = {};
  f32x16 negm = {};
  for (int t = 0; t < 64; ++t) {
    const bf16x8 kf = *(const bf16x8*)(kp + t * 1024);
    if (vlane) {
      vf0 = *(const bf16x8*)(vp + t * 1024);
      vf1 = *(const bf16x8*)(vp + t * 1024 + 512);
    }
    f32x16 s = __builtin_amdgcn_mfma_f32_32x32x16_bf16(kf, qf, negm, 0, 0, 0);
    float mx = s[0];
#pragma unroll
    for (int i = 1; i < 16; ++i) mx = fmaxf(mx, s[i]);
    if (t == 0 || __any(mx > RESCALE_THR)) {
      float d = fmaxf(mx, __shfl_xor(mx, 32));
      if (t != 0) d = fmaxf(d, 0.f);
      const float sc = (t != 0) ? __builtin_amdgcn_exp2f(-d) : 1.f;
#pragma unroll
      for (int i = 0; i < 16; ++i) { s[i] -= d; negm[i] -= d; o[i] *= sc; }
    }
    u32x4 p0, p1;
#pragma unroll
    for (int i = 0; i < 4; ++i) {
      p0[i] = cvtpk(__builtin_amdgcn_exp2f(s[2 * i]), __builtin_amdgcn_exp2f(s[2 * i + 1]));
      p1[i] = cvtpk(__builtin_amdgcn_exp2f(s[8 + 2 * i]), __builtin_amdgcn_exp2f(s[8 + 2 * i + 1]));
    }
    o = __builtin_amdgcn_mfma_f32_32x32x16_bf16(vf0, __builtin_bit_cast(bf16x8, p0), o, 0, 0, 0);
    o = __builtin_amdgcn_mfma_f32_32x32x16_bf16(vf1, __builtin_bit_cast(bf16x8, p1), o, 0, 0, 0);
  }
  const float l = __shfl(o[8], r32);
  const float inv = 1.f / l;
  short* op = (short*)(ws + WS_O) + row * 16 + 4 * h;
  u32x2 a, c;
  a[0] = cvtpk(o[0] * inv, o[1] * inv); a[1] = cvtpk(o[2] * inv, o[3] * inv);
  c[0] = cvtpk(o[4] * inv, o[5] * inv); c[1] = cvtpk(o[6] * inv, o[7] * inv);
  *(u32x2*)op = a;
  *(u32x2*)(op + 8) = c;
}

__global__ __launch_bounds__(256) void k3_wo(const short* __restrict__ x, const short* __restrict__ wo,
                                             char* __restrict__ ws) {
  extern __shared__ __attribute__((aligned(16))) char smem[];
  char* sA = smem;
  char* sB = smem + 65536;
  const int tid = threadIdx.x, lane = tid & 63, w = tid >> 6;
  const int b = blockIdx.x, xcd = b & 7, idx = b >> 3;
  const int mt = xcd * 4 + (idx & 3), nt = idx >> 2;
  const int m0 = mt * 64, n0 = nt * 64;
  const short* O = (const short*)(ws + WS_O);
  bf16x8 av[16], bv[16];
#pragma unroll
  for (int i = 0; i < 16; ++i) av[i] = *(const bf16x8*)(O + (size_t)(m0 + w * 16 + i) * DM + lane * 8);
#pragma unroll
  for (int i = 0; i < 16; ++i) {
    const int k = i * 32 + (tid >> 3), c = tid & 7;
    bv[i] = *(const bf16x8*)(wo + (size_t)k * DM + n0 + c * 8);
  }
#pragma unroll
  for (int i = 0; i < 16; ++i) {
    const int row = w * 16 + i;
    *(bf16x8*)(sA + row * 1024 + ((lane ^ (row & 15)) << 4)) = av[i];
  }
#pragma unroll
  for (int i = 0; i < 16; ++i) {
    const int k = i * 32 + (tid >> 3), c = tid & 7;
    *(bf16x8*)(sB + k * 128 + ((c << 4) ^ (((k >> 1) & 1) << 6))) = bv[i];
  }
  __syncthreads();
  const int r32 = lane & 31, h = lane >> 5, g = lane >> 4, q4 = (lane & 15) >> 2, p4 = lane & 3;
  const int wm = w >> 1, wn = w & 1;
  f32x16 acc = {};
  {
    const int arow = wm * 32 + r32;
    const char* ap = sA + arow * 1024;
    const int nb = (wn * 32 + (g & 1) * 16 + 4 * p4) * 2;
#pragma unroll 8
    for (int s = 0; s < 32; ++s) {
      const bf16x8 af = *(const bf16x8*)(ap + (((2 * s + h) ^ (arow & 15)) << 4));
      const int k0 = 16 * s + 8 * h + q4;
      const s16x4 b0 = tr_read(sB + k0 * 128 + (nb ^ (((k0 >> 1) & 1) << 6)));
      const s16x4 b1 = tr_read(sB + (k0 + 4) * 128 + (nb ^ ((((k0 + 4) >> 1) & 1) << 6)));
      acc = __builtin_amdgcn_mfma_f32_32x32x16_bf16(cat8(b0, b1), af, acc, 0, 0, 0);
    }
  }
  const size_t off = (size_t)(m0 + wm * 32 + r32) * DM + n0 + wn * 32 + 4 * h;
  short* ho = (short*)(ws + WS_H) + off;
#pragma unroll
  for (int gg = 0; gg < 4; ++gg) {
    const s16x4 xr = *(const s16x4*)(x + off + 8 * gg);
    u32x2 o;
    o[0] = cvtpk(acc[4 * gg] + bf2f(xr[0]), acc[4 * gg + 1] + bf2f(xr[1]));
    o[1] = cvtpk(acc[4 * gg + 2] + bf2f(xr[2]), acc[4 * gg + 3] + bf2f(xr[3]));
    *(u32x2*)(ho + 8 * gg) = o;
  }
}

__global__ __launch_bounds__(256) void k4_ffn1(const short* __restrict__ w1, const short* __restrict__ w3,
                                               char* __restrict__ ws) {
  extern __shared__ __attribute__((aligned(16))) char smem[];
  char* sA = smem;
  char* sB = smem + 65536;
  const int tid = threadIdx.x, lane = tid & 63, w = tid >> 6;
  const int b = blockIdx.x;
  const int mt = b & 31, nt = b >> 5;
  const int m0 = mt * 64, f0 = nt * 32;
  const short* H = (const short*)(ws + WS_H);
  bf16x8 xv[16], bv[16];
#pragma unroll
  for (int i = 0; i < 16; ++i) xv[i] = *(const bf16x8*)(H + (size_t)(m0 + w * 16 + i) * DM + lane * 8);
#pragma unroll
  for (int i = 0; i < 16; ++i) {
    const int k = i * 32 + (tid >> 3), c = tid & 7;
    const short* src = ((c >> 1) & 1) ? w3 : w1;
    bv[i] = *(const bf16x8*)(src + (size_t)k * FF + f0 + (c >> 2) * 16 + (c & 1) * 8);
  }
#pragma unroll
  for (int i = 0; i < 16; ++i) {
    float f[8];
    float s = 0.f;
#pragma unroll
    for (int j = 0; j < 8; ++j) { f[j] = bf2f(xv[i][j]); s += f[j]; }
    const float mean = wave_sum(s) * (1.f / DM);
    float q = 0.f;
#pragma unroll
    for (int j = 0; j < 8; ++j) { f[j] -= mean; q += f[j] * f[j]; }
    const float rstd = rsqrtf(wave_sum(q) * (1.f / DM) + 1e-5f);
    u32x4 o;
#pragma unroll
    for (int j = 0; j < 4; ++j) o[j] = cvtpk(f[2 * j] * rstd, f[2 * j + 1] * rstd);
    const int row = w * 16 + i;
    *(u32x4*)(sA + row * 1024 + ((lane ^ (row & 15)) << 4)) = o;
  }
#pragma unroll
  for (int i = 0; i < 16; ++i) {
    const int k = i * 32 + (tid >> 3), c = tid & 7;
    *(bf16x8*)(sB + k * 128 + ((c << 4) ^ (((k >> 1) & 1) << 6))) = bv[i];
  }
  __syncthreads();
  const int r32 = lane & 31, h = lane >> 5, g = lane >> 4, q4 = (lane & 15) >> 2, p4 = lane & 3;
  const int wm = w >> 1, wn = w & 1;
  f32x16 acc = {};
  {
    const int arow = wm * 32 + r32;
    const char* ap = sA + arow * 1024;
    const int nb = (wn * 32 + (g & 1) * 16 + 4 * p4) * 2;
#pragma unroll 8
    for (int s = 0; s < 32; ++s) {
      const bf16x8 af = *(const bf16x8*)(ap + (((2 * s + h) ^ (arow & 15)) << 4));
      const int k0 = 16 * s + 8 * h + q4;
      const s16x4 b0 = tr_read(sB + k0 * 128 + (nb ^ (((k0 >> 1) & 1) << 6)));
      const s16x4 b1 = tr_read(sB + (k0 + 4) * 128 + (nb ^ ((((k0 + 4) >> 1) & 1) << 6)));
      acc = __builtin_amdgcn_mfma_f32_32x32x16_bf16(cat8(b0, b1), af, acc, 0, 0, 0);
    }
  }
  short* go = (short*)(ws + WS_G) + (size_t)(m0 + wm * 32 + r32) * FF + f0 + wn * 16 + 4 * h;
  float r[8];
#pragma unroll
  for (int i = 0; i < 8; ++i) {
    const float gt = acc[i];
    r[i] = gt / (1.f + __expf(-gt)) * acc[i + 8];
  }
  u32x2 a, c;
  a[0] = cvtpk(r[0], r[1]); a[1] = cvtpk(r[2], r[3]);
  c[0] = cvtpk(r[4], r[5]); c[1] = cvtpk(r[6], r[7]);
  *(u32x2*)go = a;
  *(u32x2*)(go + 8) = c;
}

__global__ __launch_bounds__(256) void k5_ffn2(const short* __restrict__ w2, char* __restrict__ ws,
                                               short* __restrict__ out) {
  extern __shared__ __attribute__((aligned(16))) char smem[];
  char* sA = smem;
  char* sB = smem + 32768;
  const int tid = threadIdx.x, lane = tid & 63, w = tid >> 6;
  const int b = blockIdx.x, xcd = b & 7, idx = b >> 3;
  const int mt = xcd * 4 + (idx & 3), nt = idx >> 2;
  const int m0 = mt * 64, n0 = nt * 64;
  const short* G = (const short*)(ws + WS_G);
  const int r32 = lane & 31, h = lane >> 5, g = lane >> 4, q4 = (lane & 15) >> 2, p4 = lane & 3;
  const int wm = w >> 1, wn = w & 1;
  f32x16 acc = {};
  for (int kc = 0; kc < FF; kc += 256) {
    const int klen = (FF - kc) < 256 ? (FF - kc) : 256;
    bf16x8 av[8], bv[8];
#pragma unroll
    for (int i = 0; i < 8; ++i) {
      const int row = i * 8 + (tid >> 5), c = tid & 31;
      av[i] = (c * 8 < klen) ? *(const bf16x8*)(G + (size_t)(m0 + row) * FF + kc + c * 8) : bf16x8{};
    }
#pragma unroll
    for (int i = 0; i < 8; ++i) {
      const int k = i * 32 + (tid >> 3), c = tid & 7;
      bv[i] = (k < klen) ? *(const bf16x8*)(w2 + (size_t)(kc + k) * DM + n0 + c * 8) : bf16x8{};
    }
    __syncthreads();
#pragma unroll
    for (int i = 0; i < 8; ++i) {
      const int row = i * 8 + (tid >> 5), c = tid & 31;
      *(bf16x8*)(sA + row * 512 + ((c ^ (row & 15)) << 4)) = av[i];
    }
#pragma unroll
    for (int i = 0; i < 8; ++i) {
      const int k = i * 32 + (tid >> 3), c = tid & 7;
      *(bf16x8*)(sB + k * 128 + ((c << 4) ^ (((k >> 1) & 1) << 6))) = bv[i];
    }
    __syncthreads();
    const int arow = wm * 32 + r32;
    const char* ap = sA + arow * 512;
    const int nb = (wn * 32 + (g & 1) * 16 + 4 * p4) * 2;
    const int ns = klen >> 4;
    for (int s = 0; s < ns; ++s) {
      const bf16x8 af = *(const bf16x8*)(ap + (((2 * s + h) ^ (arow & 15)) << 4));
      const int k0 = 16 * s + 8 * h + q4;
      const s16x4 b0 = tr_read(sB + k0 * 128 + (nb ^ (((k0 >> 1) & 1) << 6)));
      const s16x4 b1 = tr_read(sB + (k0 + 4) * 128 + (nb ^ ((((k0 + 4) >> 1) & 1) << 6)));
      acc = __builtin_amdgcn_mfma_f32_32x32x16_bf16(cat8(b0, b1), af, acc, 0, 0, 0);
    }
  }
  const size_t off = (size_t)(m0 + wm * 32 + r32) * DM + n0 + wn * 32 + 4 * h;
  const short* H = (const short*)(ws + WS_H);
#pragma unroll
  for (int gg = 0; gg < 4; ++gg) {
    const s16x4 xr = *(const s16x4*)(H + off + 8 * gg);
    u32x2 o;
    o[0] = cvtpk(acc[4 * gg] + bf2f(xr[0]), acc[4 * gg + 1] + bf2f(xr[1]));
    o[1] = cvtpk(acc[4 * gg + 2] + bf2f(xr[2]), acc[4 * gg + 3] + bf2f(xr[3]));
    *(u32x2*)(out + off + 8 * gg) = o;
  }
}

extern "C" void kernel_launch(void* const* d_in, const int* in_sizes, int n_in, void* d_out, int out_size,
                              void* d_ws, size_t ws_size, hipStream_t stream) {
  const short* x = (const short*)d_in[0];
  const short* wq = (const short*)d_in[1];
  const short* wk = (const short*)d_in[2];
  const short* wv = (const short*)d_in[3];
  const short* wo = (const short*)d_in[4];
  const short* w1 = (const short*)d_in[6];
  const short* w3 = (const short*)d_in[7];
  const short* w2 = (const short*)d_in[8];
  char* ws = (char*)d_ws;
  hipFuncSetAttribute((const void*)k1_ln_qkv, hipFuncAttributeMaxDynamicSharedMemorySize, 147456);
  hipFuncSetAttribute((const void*)k2_attn, hipFuncAttributeMaxDynamicSharedMemorySize, 131072);
  hipFuncSetAttribute((const void*)k3_wo, hipFuncAttributeMaxDynamicSharedMemorySize, 131072);
  hipFuncSetAttribute((const void*)k4_ffn1, hipFuncAttributeMaxDynamicSharedMemorySize, 131072);
  hipFuncSetAttribute((const void*)k5_ffn2, hipFuncAttributeMaxDynamicSharedMemorySize, 65536);
  k1_ln_qkv<<<256, 256, 147456, stream>>>(x, wq, wk, wv, ws);
  k2_attn<<<256, 512, 131072, stream>>>(ws);
  k3_wo<<<256, 256, 131072, stream>>>(x, wo, ws);
  k4_ffn1<<<32 * 43, 256, 131072, stream>>>(w1, w3, ws);
  k5_ffn2<<<256, 256, 65536, stream>>>(w2, ws, (short*)d_out);
}
```

```cpp
#include <hip/hip_runtime.h>
#include <hip/hip_bf16.h>

typedef __attribute__((ext_vector_type(8))) short bf16x8;
typedef __attribute__((ext_vector_type(4))) short s16x4;
typedef __attribute__((ext_vector_type(16))) float f32x16;
typedef __attribute__((ext_vector_type(4))) float f32x4;
typedef __attribute__((ext_vector_type(2))) float f32x2;
typedef __attribute__((ext_vector_type(4))) unsigned u32x4;
typedef __attribute__((ext_vector_type(2))) unsigned u32x2;
typedef __attribute__((ext_vector_type(2))) __bf16 bf16x2_t;
typedef __attribute__((address_space(3))) s16x4 lds_s16x4;

#define DI __device__ __forceinline__

constexpr int S_LEN = 2048, DM = 512, NH = 32, DH = 16, FF = 1376;
constexpr float QSCALE = 0.25f * 1.4426950408889634f;

constexpr size_t WS_Q = 0;
constexpr size_t WS_KF = WS_Q + (size_t)S_LEN * DM * 2;
constexpr size_t WS_VF = WS_KF + (size_t)S_LEN * DH * 2;
constexpr size_t WS_O = WS_VF + (size_t)S_LEN * DH * 2;
constexpr size_t WS_H = WS_O + (size_t)S_LEN * DM * 2;
constexpr size_t WS_G = WS_H + (size_t)S_LEN * DM * 2;
constexpr size_t WS_KM = WS_G + (size_t)S_LEN * FF * 2;
constexpr size_t WS_ST = WS_KM + 512;
constexpr size_t WS_CS = WS_ST + (size_t)S_LEN * 32 * 4;

DI unsigned cvtpk(float lo, float hi) {
  const f32x2 v = {lo, hi};
  return __builtin_bit_cast(unsigned, __builtin_convertvector(v, bf16x2_t));
}
DI float bf2f(short v) { return __uint_as_float(((unsigned)(unsigned short)v) << 16); }
DI float bflo(unsigned u) { return __uint_as_float(u << 16); }
DI float bfhi(unsigned u) { return __uint_as_float(u & 0xffff0000u); }
DI s16x4 tr_read(const char* p) {
  return __builtin_amdgcn_ds_read_tr16_b64_v4i16((lds_s16x4*)p);
}
DI bf16x8 cat8(s16x4 a, s16x4 b) { return __builtin_shufflevector(a, b, 0, 1, 2, 3, 4, 5, 6, 7); }
DI void stat2(unsigned u, float& s, float& q) {
  const float a = bflo(u), b = bfhi(u);
  s += a + b;
  q = fmaf(a, a, fmaf(b, b, q));
}
DI void stat8(const bf16x8& v, float& s, float& q) {
  const u32x4 u = __builtin_bit_cast(u32x4, v);
#pragma unroll
  for (int j = 0; j < 4; ++j) stat2(u[j], s, q);
}
#define TS(kid, i)

constexpr int PD = 3;

constexpr int K1_ST = 36864;
constexpr int K1_LDS = 2 * K1_ST + 1024;
__global__ __launch_bounds__(512) void k1_ln_qkv(const short* __restrict__ x, const short* __restrict__ wq,
                                                 const short* __restrict__ wk, const short* __restrict__ wv,
                                                 char* __restrict__ ws) {
  extern __shared__ __attribute__((aligned(16))) char smem[];
  float* sstat = (float*)(smem + 2 * K1_ST);
  const int tid = threadIdx.x, lane = tid & 63, w = tid >> 6;
  const int b = blockIdx.x, xcd = b & 7, idx = b >> 3;
  const int mt = xcd * 4 + (idx & 3), nt = idx >> 2;
  const int m0 = mt * 64, n0 = nt * 64;
  const int ar = tid >> 4, ac = tid & 15, bk = tid >> 3, bc = tid & 7;
  const short* wkv = (nt & 1) ? wv : wk;
  TS(0, 0);
  bf16x8 av[PD][2], bv[PD][2], cv[PD];
#define K1_LOAD(c) do { _Pragma("unroll") for (int i = 0; i < 2; ++i) { \
      av[(c) % PD][i] = *(const bf16x8*)(x + (size_t)(m0 + i * 32 + ar) * DM + (c) * 128 + ac * 8); \
      bv[(c) % PD][i] = *(const bf16x8*)(wq + (size_t)((c) * 128 + i * 64 + bk) * DM + n0 + bc * 8); } \
    cv[(c) % PD] = *(const bf16x8*)(wkv + (size_t)(c) * 2048 + (tid & 255) * 8); } while (0)
#define K1_WRITE(c) do { char* sA_ = smem + ((c) & 1) * K1_ST; _Pragma("unroll") for (int i = 0; i < 2; ++i) { \
      const int row = i * 32 + ar; \
      *(bf16x8*)(sA_ + row * 256 + ((ac ^ (row & 15)) << 4)) = av[(c) % PD][i]; \
      stat8(av[(c) % PD][i], ssum[i], ssq[i]); \
      const int k = i * 64 + bk; \
      *(bf16x8*)(sA_ + 16384 + k * 128 + ((bc << 4) ^ (((k >> 1) & 1) << 6))) = bv[(c) % PD][i]; } \
    *(bf16x8*)(sA_ + 32768 + (tid & 255) * 16) = cv[(c) % PD]; } while (0)
  float ssum[2] = {0.f, 0.f}, ssq[2] = {0.f, 0.f};
  K1_LOAD(0); K1_LOAD(1); K1_LOAD(2);
  K1_WRITE(0); K1_LOAD(3);

  const int r32 = lane & 31, h = lane >> 5, g = lane >> 4, q4 = (lane & 15) >> 2, p4 = lane & 3;
  const int wq_ = w & 3, kp = w >> 2;
  const int wm = wq_ >> 1, wn = wq_ & 1;
  const int arow = wm * 32 + r32;
  const int nb = (wn * 32 + (g & 1) * 16 + 4 * p4) * 2;
  const int rb = (nt >> 1) * 16;
  bf16x8 ones;
#pragma unroll
  for (int j = 0; j < 8; ++j) ones[j] = (short)0x3F80;
  f32x16 acc = {}, cs = {};
  f32x4 c4 = {}, cs4 = {};
#pragma unroll
  for (int c = 0; c < 4; ++c) {
    __syncthreads();
    if (c == 0) TS(0, 1);
    const char* sA = smem + (c & 1) * K1_ST;
    const char* sB = sA + 16384;
    bf16x8 af[4], bf[4];
#pragma unroll
    for (int ss = 0; ss < 4; ++ss) {
      const int s = 2 * ss + kp;
      af[ss] = *(const bf16x8*)(sA + arow * 256 + (((2 * s + h) ^ (arow & 15)) << 4));
      const int k0 = 16 * s + 8 * h + q4;
      bf[ss] = cat8(tr_read(sB + k0 * 128 + (nb ^ (((k0 >> 1) & 1) << 6))),
                    tr_read(sB + (k0 + 4) * 128 + (nb ^ ((((k0 + 4) >> 1) & 1) << 6))));
    }
    if (c < 3) { K1_WRITE(c + 1); }
#pragma unroll
    for (int ss = 0; ss < 4; ++ss) {
      acc = __builtin_amdgcn_mfma_f32_32x32x16_bf16(bf[ss], af[ss], acc, 0, 0, 0);
      cs = __builtin_amdgcn_mfma_f32_32x32x16_bf16(bf[ss], ones, cs, 0, 0, 0);
    }
    if (w == 5) {
      const char* sC = sA + 32768;
      const int krow = rb + (lane & 15);
#pragma unroll
      for (int s = 0; s < 4; ++s) {
        const bf16x8 a16 = *(const bf16x8*)(sA + krow * 256 + (((4 * s + g) ^ (krow & 15)) << 4));
        const int k0 = 32 * s + 8 * g + q4;
        const bf16x8 b16 = cat8(tr_read(sC + k0 * 32 + 8 * p4), tr_read(sC + (k0 + 4) * 32 + 8 * p4));
        if (nt & 1) {
          c4 = __builtin_amdgcn_mfma_f32_16x16x32_bf16(a16, b16, c4, 0, 0, 0);
          cs4 = __builtin_amdgcn_mfma_f32_16x16x32_bf16(ones, b16, cs4, 0, 0, 0);
        } else {
          c4 = __builtin_amdgcn_mfma_f32_16x16x32_bf16(b16, a16, c4, 0, 0, 0);
          cs4 = __builtin_amdgcn_mfma_f32_16x16x32_bf16(b16, ones, cs4, 0, 0, 0);
        }
      }
    }
  }
  TS(0, 2);
#pragma unroll
  for (int i = 0; i < 2; ++i) {
    float s_ = ssum[i], q_ = ssq[i];
#pragma unroll
    for (int o = 1; o < 16; o <<= 1) { s_ += __shfl_xor(s_, o); q_ += __shfl_xor(q_, o); }
    if (ac == 0) {
      const float mean = s_ * (1.f / DM);
      const float var = fmaxf(q_ * (1.f / DM) - mean * mean, 0.f);
      sstat[(i * 32 + ar) * 2] = mean;
      sstat[(i * 32 + ar) * 2 + 1] = rsqrtf(var + 1e-5f);
    }
  }
  __syncthreads();
  const float mean = sstat[arow * 2], rstd = sstat[arow * 2 + 1];
#pragma unroll
  for (int i = 0; i < 16; ++i) acc[i] = fmaf(-mean, cs[i], acc[i]);
  float* red = (float*)smem + wq_ * 1024 + lane;
  if (kp == 1) {
#pragma unroll
    for (int i = 0; i < 16; ++i) red[i * 64] = acc[i];
  }
  if (w == 5) {
    float r[4];
    u32x2 o;
    if (nt & 1) {
#pragma unroll
      for (int i = 0; i < 4; ++i) {
        const int rr = rb + 4 * g + i;
        r[i] = (c4[i] - sstat[rr * 2] * cs4[i]) * sstat[rr * 2 + 1];
      }
      o[0] = cvtpk(r[0], r[1]); o[1] = cvtpk(r[2], r[3]);
      const int key0 = m0 + rb;
      const int t = key0 >> 5, s = (key0 >> 4) & 1;
      short* vf = (short*)(ws + WS_VF) + (size_t)t * 512 + s * 256 + (g & 1) * 128 + (lane & 15) * 8 + (g >> 1) * 4;
      *(u32x2*)vf = o;
    } else {
      const int rr = rb + (lane & 15);
      const float mn = sstat[rr * 2], rs = sstat[rr * 2 + 1];
#pragma unroll
      for (int i = 0; i < 4; ++i) r[i] = (c4[i] - mn * cs4[i]) * rs;
      o[0] = cvtpk(r[0], r[1]); o[1] = cvtpk(r[2], r[3]);
      const int key = m0 + rr;
      const int t = key >> 5;
      short* kf = (short*)(ws + WS_KF) + (size_t)t * 512 + (g >> 1) * 256 + (key & 31) * 8 + (g & 1) * 4;
      *(u32x2*)kf = o;
      const float k0 = bflo(o[0]), k1 = bfhi(o[0]), k2 = bflo(o[1]), k3 = bfhi(o[1]);
      float a = k0 * k0 + k1 * k1 + k2 * k2 + k3 * k3;
      a += __shfl_xor(a, 16);
      a += __shfl_xor(a, 32);
#pragma unroll
      for (int of = 8; of > 0; of >>= 1) a = fmaxf(a, __shfl_xor(a, of));
      if (lane == 0) ((float*)(ws + WS_KM))[mt * 4 + (nt >> 1)] = a;
    }
  }
  __syncthreads();
  if (kp == 1) return;
  const float sc = rstd * QSCALE;
  short* qo = (short*)(ws + WS_Q) + (size_t)(m0 + arow) * DM + n0 + wn * 32 + 4 * h;
#pragma unroll
  for (int gg = 0; gg < 4; ++gg) {
    u32x2 o;
    o[0] = cvtpk((acc[4 * gg] + red[(4 * gg) * 64]) * sc, (acc[4 * gg + 1] + red[(4 * gg + 1) * 64]) * sc);
    o[1] = cvtpk((acc[4 * gg + 2] + red[(4 * gg + 2) * 64]) * sc, (acc[4 * gg + 3] + red[(4 * gg + 3) * 64]) * sc);
    *(u32x2*)(qo + 8 * gg) = o;
  }
  TS(0, 3);
#undef K1_LOAD
#undef K1_WRITE
}

constexpr int TS = 2080;
constexpr int ATT_RED = 64 * TS;
constexpr int ATT_LDS = ATT_RED + 8 * 64 * 9 * 4 + 1024;

__global__ __launch_bounds__(1024) void k2_attn(const short* __restrict__ w1, const short* __restrict__ w3,
                                                const short* __restrict__ wo, char* __restrict__ ws) {
  extern __shared__ __attribute__((aligned(16))) char smem[];
  const int tid = threadIdx.x, lane = tid & 63, w = tid >> 6;
  const int r32 = lane & 31, h = lane >> 5;
  float* scomb = (float*)(smem + ATT_RED);
  const int wr = w & 7, kh = w >> 3;
  const int pblk = (blockIdx.x & 7) * 32 + (blockIdx.x >> 3);
  const size_t row = (size_t)pblk * 256 + wr * 32 + r32;
  const int tl0 = (w < 8) ? w : 24 + w;
  bf16x8 kv[8];
  {
    const bf16x8* srck = (const bf16x8*)(ws + WS_KF) + tl0 * 64 + lane;
    const bf16x8* srcv = (const bf16x8*)(ws + WS_VF) + tl0 * 64 + lane;
#pragma unroll
    for (int j = 0; j < 4; ++j) { kv[2 * j] = srck[j * 512]; kv[2 * j + 1] = srcv[j * 512]; }
  }
  const bf16x8 qf = *(const bf16x8*)((const short*)(ws + WS_Q) + row * 16 + h * 8);
  bf16x8 csv = {};
  if (blockIdx.x < 172) {
    const int gi = blockIdx.x;
    const short* wsrc = (gi < 86) ? (w1 + gi * 16) : (w3 + (gi - 86) * 16);
    csv = *(const bf16x8*)(wsrc + (size_t)(tid >> 1) * FF + (tid & 1) * 8);
  }
  float kn2 = fmaxf(((const float*)(ws + WS_KM))[lane], ((const float*)(ws + WS_KM))[64 + lane]);
  if (tid < 512) *(unsigned*)(smem + (tid >> 3) * TS + 2048 + (tid & 7) * 4) = (tid & 7) < 4 ? 0u : 0x3F803F80u;
  char* sdst = smem + tl0 * TS + lane * 16;
  *(bf16x8*)(sdst) = kv[0];
  *(bf16x8*)(sdst + 1024) = kv[1];
  float* scs = scomb + 8 * 64 * 9;
  if (blockIdx.x < 172) {
    float cf[8];
#pragma unroll
    for (int j = 0; j < 8; ++j) {
      cf[j] = bf2f(csv[j]);
#pragma unroll
      for (int of = 2; of < 64; of <<= 1) cf[j] += __shfl_xor(cf[j], of);
    }
    if (lane < 2) {
#pragma unroll
      for (int j = 0; j < 8; ++j) scs[w * 16 + lane * 8 + j] = cf[j];
    }
  }
#pragma unroll
  for (int o = 32; o > 0; o >>= 1) kn2 = fmaxf(kn2, __shfl_xor(kn2, o));
  float qn2 = 0.f;
#pragma unroll
  for (int j = 0; j < 8; ++j) { const float f = bf2f(qf[j]); qn2 += f * f; }
  qn2 += __shfl_xor(qn2, 32);
  const float mref = sqrtf(qn2 * kn2) * 1.001f;

  const bool vlane = r32 < 16;
  const int kbase = lane * 16;
  const int vbase0 = vlane ? (1024 + h * 256 + r32 * 16) : (2048 + (r32 == 16 ? 16 : 0));
  const int vbase1 = vlane ? vbase0 + 512 : vbase0;
  f32x16 o = {};
  {
    f32x16 negm;
#pragma unroll
    for (int i = 0; i < 16; ++i) negm[i] = -mref;
    const char* kp = smem + kbase + kh * 32 * TS;
    const char* vp0 = smem + vbase0 + kh * 32 * TS;
    const char* vp1 = smem + vbase1 + kh * 32 * TS;
    u32x4 pf[2] = {};
#pragma unroll
    for (int j = 0; j < 4; ++j) {
      __syncthreads();
      if (j < 3) {
        *(bf16x8*)(sdst + (j + 1) * 8 * TS) = kv[2 * j + 2];
        *(bf16x8*)(sdst + (j + 1) * 8 * TS + 1024) = kv[2 * j + 3];
      }
      asm volatile("" :: "v"(pf[0]), "v"(pf[1]));
#pragma unroll
      for (int i = 0; i < 2; ++i) {
        int pi = ((blockIdx.x >> 3) * 8 + j * 2 + i) * 816 + tid;
        pi = (tid < 816) ? pi : 0;
        const short* src = pi < 32768 ? wo + (size_t)pi * 8 : (pi < 120832 ? w1 + (size_t)(pi - 32768) * 8 : w3 + (size_t)(pi - 120832) * 8);
        pf[i] = *(const u32x4*)src;
      }
#pragma unroll
      for (int t = j * 8; t < j * 8 + 8; ++t) {
        const bf16x8 kf = *(const bf16x8*)(kp + t * TS);
        const bf16x8 vf0 = *(const bf16x8*)(vp0 + t * TS);
        const bf16x8 vf1 = *(const bf16x8*)(vp1 + t * TS);
        const f32x16 s = __builtin_amdgcn_mfma_f32_32x32x16_bf16(kf, qf, negm, 0, 0, 0);
        u32x4 p0, p1;
#pragma unroll
        for (int i = 0; i < 4; ++i) {
          p0[i] = cvtpk(__builtin_amdgcn_exp2f(s[2 * i]), __builtin_amdgcn_exp2f(s[2 * i + 1]));
          p1[i] = cvtpk(__builtin_amdgcn_exp2f(s[8 + 2 * i]), __builtin_amdgcn_exp2f(s[8 + 2 * i + 1]));
        }
        o = __builtin_amdgcn_mfma_f32_32x32x16_bf16(vf0, __builtin_bit_cast(bf16x8, p0), o, 0, 0, 0);
        o = __builtin_amdgcn_mfma_f32_32x32x16_bf16(vf1, __builtin_bit_cast(bf16x8, p1), o, 0, 0, 0);
      }
    }
    asm volatile("" :: "v"(pf[0]), "v"(pf[1]));
  }
  float* cb = scomb + wr * (64 * 9) + lane * 9;
  if (kh == 1) {
#pragma unroll
    for (int i = 0; i < 9; ++i) cb[i] = o[i];
  }
  __syncthreads();
  if (blockIdx.x < 172 && tid >= 1008) {
    float a = 0.f;
#pragma unroll
    for (int i = 0; i < 16; ++i) a += scs[i * 16 + (tid - 1008)];
    ((float*)(ws + WS_CS))[blockIdx.x * 16 + (tid - 1008)] = a;
  }
  if (kh == 1) return;
#pragma unroll
  for (int i = 0; i < 9; ++i) o[i] += cb[i];
  float l = __shfl(o[8], r32);
  if (__any(l < 1e-18f)) {
    f32x16 negm = {};
    o = f32x16{};
#pragma unroll 1
    for (int t = 0; t < 64; ++t) {
      const bf16x8 kf = *(const bf16x8*)(smem + kbase + t * TS);
      const bf16x8 vf0 = *(const bf16x8*)(smem + vbase0 + t * TS);
      const bf16x8 vf1 = *(const bf16x8*)(smem + vbase1 + t * TS);
      f32x16 s = __builtin_amdgcn_mfma_f32_32x32x16_bf16(kf, qf, negm, 0, 0, 0);
      float mx = s[0];
#pragma unroll
      for (int i = 1; i < 16; ++i) mx = fmaxf(mx, s[i]);
      float d = fmaxf(mx, __shfl_xor(mx, 32));
      if (t != 0) d = fmaxf(d, 0.f);
      const float sc = (t != 0) ? __builtin_amdgcn_exp2f(-d) : 1.f;
#pragma unroll
      for (int i = 0; i < 16; ++i) { s[i] -= d; negm[i] -= d; o[i] *= sc; }
      u32x4 p0, p1;
#pragma unroll
      for (int i = 0; i < 4; ++i) {
        p0[i] = cvtpk(__builtin_amdgcn_exp2f(s[2 * i]), __builtin_amdgcn_exp2f(s[2 * i + 1]));
        p1[i] = cvtpk(__builtin_amdgcn_exp2f(s[8 + 2 * i]), __builtin_amdgcn_exp2f(s[8 + 2 * i + 1]));
      }
      o = __builtin_amdgcn_mfma_f32_32x32x16_bf16(vf0, __builtin_bit_cast(bf16x8, p0), o, 0, 0, 0);
      o = __builtin_amdgcn_mfma_f32_32x32x16_bf16(vf1, __builtin_bit_cast(bf16x8, p1), o, 0, 0, 0);
    }
    l = __shfl(o[8], r32);
  }
  const float inv = 1.f / l;
  short* op = (short*)(ws + WS_O) + row * 16 + 4 * h;
  u32x2 a, c;
  a[0] = cvtpk(o[0] * inv, o[1] * inv); a[1] = cvtpk(o[2] * inv, o[3] * inv);
  c[0] = cvtpk(o[4] * inv, o[5] * inv); c[1] = cvtpk(o[6] * inv, o[7] * inv);
  *(u32x2*)op = a;
  *(u32x2*)(op + 8) = c;
}

constexpr int KG_LDS = 2 * 32768;
template <int KTOT, bool STATS>
__global__ __launch_bounds__(512) void k_gemm64(const short* __restrict__ A, const short* __restrict__ B,
                                                const short* __restrict__ resid, short* __restrict__ out,
                                                char* __restrict__ ws) {
  extern __shared__ __attribute__((aligned(16))) char smem[];
  constexpr int NCH = (KTOT + 127) / 128;
  constexpr int KLAST = KTOT - (NCH - 1) * 128;
  const int tid = threadIdx.x, lane = tid & 63, w = tid >> 6;
  const int b = blockIdx.x, xcd = b & 7, idx = b >> 3;
  const int mt = xcd * 4 + (idx & 3), nt = idx >> 2;
  const int m0 = mt * 64, n0 = nt * 64;
  const int ar = tid >> 4, ac = tid & 15, bk = tid >> 3, bc = tid & 7;
  TS((STATS ? 2 : 4), 0);
  bf16x8 av[PD][2], bv[PD][2];
#define KG_LOAD(c) do { _Pragma("unroll") for (int i = 0; i < 2; ++i) { \
      const int ka = (((c) < NCH - 1) || (ac * 8 < KLAST)) ? (c) * 128 + ac * 8 : 0; \
      av[(c) % PD][i] = *(const bf16x8*)(A + (size_t)(m0 + i * 32 + ar) * KTOT + ka); \
      const int kb = (((c) < NCH - 1) || (i * 64 + bk < KLAST)) ? (c) * 128 + i * 64 + bk : 0; \
      bv[(c) % PD][i] = *(const bf16x8*)(B + (size_t)kb * DM + n0 + bc * 8); } } while (0)
#define KG_WRITE(c) do { char* sA_ = smem + ((c) & 1) * 32768; _Pragma("unroll") for (int i = 0; i < 2; ++i) { \
      const int row = i * 32 + ar; \
      *(bf16x8*)(sA_ + row * 256 + ((ac ^ (row & 15)) << 4)) = av[(c) % PD][i]; \
      const int k = i * 64 + bk; \
      *(bf16x8*)(sA_ + 16384 + k * 128 + ((bc << 4) ^ (((k >> 1) & 1) << 6))) = bv[(c) % PD][i]; } } while (0)
  KG_LOAD(0); KG_LOAD(1); KG_LOAD(2);
  KG_WRITE(0); KG_LOAD(3);

  const int r32 = lane & 31, h = lane >> 5, g = lane >> 4, q4 = (lane & 15) >> 2, p4 = lane & 3;
  const int wq_ = w & 3, kp = w >> 2;
  const int wm = wq_ >> 1, wn = wq_ & 1;
  const int arow = wm * 32 + r32;
  const int nb = (wn * 32 + (g & 1) * 16 + 4 * p4) * 2;
  const size_t off = (size_t)(m0 + arow) * DM + n0 + wn * 32 + 4 * h;
  s16x4 xr[4];
  if (kp == 0) {
#pragma unroll
    for (int gg = 0; gg < 4; ++gg) xr[gg] = *(const s16x4*)(resid + off + 8 * gg);
  }
  f32x16 acc = {};
#pragma unroll
  for (int c = 0; c < NCH; ++c) {
    __syncthreads();
    if (c == 0) TS((STATS ? 2 : 4), 1);
    const char* sA = smem + (c & 1) * 32768;
    const char* sB = sA + 16384;
    bf16x8 af[4], bf[4];
#pragma unroll
    for (int ss = 0; ss < 4; ++ss) {
      const int s = 2 * ss + kp;
      if (c < NCH - 1 || 16 * (2 * ss) < KLAST) {
        af[ss] = *(const bf16x8*)(sA + arow * 256 + (((2 * s + h) ^ (arow & 15)) << 4));
        const int k0 = 16 * s + 8 * h + q4;
        bf[ss] = cat8(tr_read(sB + k0 * 128 + (nb ^ (((k0 >> 1) & 1) << 6))),
                      tr_read(sB + (k0 + 4) * 128 + (nb ^ ((((k0 + 4) >> 1) & 1) << 6))));
      }
    }
    if (c + 1 < NCH) { KG_WRITE(c + 1); if (c + 1 + PD < NCH) KG_LOAD(c + 1 + PD); }
#pragma unroll
    for (int ss = 0; ss < 4; ++ss)
      if (c < NCH - 1 || 16 * (2 * ss) < KLAST)
        acc = __builtin_amdgcn_mfma_f32_32x32x16_bf16(bf[ss], af[ss], acc, 0, 0, 0);
  }
  TS((STATS ? 2 : 4), 2);
  float* red = (float*)(smem + (NCH & 1) * 32768) + wq_ * 1024 + lane;
  if (kp == 1) {
#pragma unroll
    for (int i = 0; i < 16; ++i) red[i * 64] = acc[i];
  }
  __syncthreads();
  if (kp == 1) return;
  float ssum = 0.f, ssq = 0.f;
#pragma unroll
  for (int gg = 0; gg < 4; ++gg) {
    u32x2 o;
    o[0] = cvtpk(acc[4 * gg] + red[(4 * gg) * 64] + bf2f(xr[gg][0]), acc[4 * gg + 1] + red[(4 * gg + 1) * 64] + bf2f(xr[gg][1]));
    o[1] = cvtpk(acc[4 * gg + 2] + red[(4 * gg + 2) * 64] + bf2f(xr[gg][2]), acc[4 * gg + 3] + red[(4 * gg + 3) * 64] + bf2f(xr[gg][3]));
    *(u32x2*)(out + off + 8 * gg) = o;
    if (STATS) {
      stat2(o[0], ssum, ssq);
      stat2(o[1], ssum, ssq);
    }
  }
  if (STATS) {
    ssum += __shfl_xor(ssum, 32);
    ssq += __shfl_xor(ssq, 32);
    if (h == 0) {
      f32x2 st = {ssum, ssq};
      *(f32x2*)((float*)(ws + WS_ST) + (size_t)(m0 + arow) * 32 + (nt * 2 + wn) * 2) = st;
    }
  }
  TS((STATS ? 2 : 4), 3);
#undef KG_LOAD
#undef KG_WRITE
}

constexpr int K4_ST = 40960;
constexpr int K4_LDS = 2 * K4_ST + 1024 + 1024;
__global__ __launch_bounds__(512) void k4_ffn1(const short* __restrict__ w1, const short* __restrict__ w3,
                                               char* __restrict__ ws) {
  extern __shared__ __attribute__((aligned(16))) char smem[];
  float* sstat = (float*)(smem + 2 * K4_ST);
  const int tid = threadIdx.x, lane = tid & 63, w = tid >> 6;
  const int b = blockIdx.x, xcd = b & 7, idx = b >> 3;
  const int mt = xcd * 2 + (idx & 1), ng = idx >> 1;
  const int m0 = mt * 128;
  const int cnt = ng < 6 ? 6 : 5;
  const int f0 = (ng * 5 + (ng < 6 ? ng : 6)) * 16;
  const short* H = (const short*)(ws + WS_H);
  TS(3, 0);
  const int ar = tid >> 3, ac = tid & 7;
  int pk[3], pd[3];
  const short* psrc[3];
#pragma unroll
  for (int i = 0; i < 3; ++i) {
    const int p = i * 512 + tid;
    pk[i] = p / 24;
    const int cc = p % 24;
    const int ct = (cc >> 2) < cnt ? (cc >> 2) : 0;
    psrc[i] = ((cc & 2) ? w3 : w1) + (size_t)pk[i] * FF + f0 + ct * 16 + (cc & 1) * 8;
    pd[i] = 16384 + pk[i] * 384 + ((cc << 4) ^ (((pk[i] >> 1) & 1) << 6));
  }
  bf16x8 av[PD][2], bv[PD][3];
#define K4_LOAD(c) do { _Pragma("unroll") for (int i = 0; i < 2; ++i) \
      av[(c) % PD][i] = *(const bf16x8*)(H + (size_t)(m0 + i * 64 + ar) * DM + (c) * 64 + ac * 8); \
    _Pragma("unroll") for (int i = 0; i < 3; ++i) \
      bv[(c) % PD][i] = *(const bf16x8*)(psrc[i] + (size_t)(c) * 64 * FF); } while (0)
#define K4_WRITE(c) do { char* sA_ = smem + ((c) & 1) * K4_ST; _Pragma("unroll") for (int i = 0; i < 2; ++i) { \
      const int row = i * 64 + ar; \
      *(bf16x8*)(sA_ + row * 128 + ((ac ^ ((row >> 1) & 7)) << 4)) = av[(c) % PD][i]; } \
    _Pragma("unroll") for (int i = 0; i < 3; ++i) *(bf16x8*)(sA_ + pd[i]) = bv[(c) % PD][i]; } while (0)
  K4_LOAD(0); K4_LOAD(1); K4_LOAD(2);
  if (tid < 128) {
    const f32x4* sp = (const f32x4*)((const float*)(ws + WS_ST) + (size_t)(m0 + tid) * 32);
    float s_ = 0.f, q_ = 0.f;
#pragma unroll
    for (int i = 0; i < 8; ++i) { const f32x4 v = sp[i]; s_ += v[0] + v[2]; q_ += v[1] + v[3]; }
    const float mean = s_ * (1.f / DM);
    const float var = fmaxf(q_ * (1.f / DM) - mean * mean, 0.f);
    sstat[tid * 2] = mean;
    sstat[tid * 2 + 1] = rsqrtf(var + 1e-5f);
  }
  float* scs = sstat + 256;
  if (tid >= 256 && tid < 448) {
    const int e = tid - 256, ct = e >> 5, isup = (e >> 4) & 1;
    const int fc = f0 + (ct < cnt ? ct : 0) * 16 + (e & 15);
    scs[e] = ((const float*)(ws + WS_CS))[isup * FF + fc];
  }
  K4_WRITE(0); K4_LOAD(3);

  const int r32 = lane & 31, h = lane >> 5, g = lane >> 4, q4 = (lane & 15) >> 2, p4 = lane & 3;
  const int wm = w >> 1, wn = w & 1;
  const int arow = wm * 32 + r32;
  const int nb = (wn * 96 + (g & 1) * 16 + 4 * p4) * 2;
  f32x16 acc[3] = {};
#pragma unroll
  for (int c = 0; c < 8; ++c) {
    __syncthreads();
    if (c == 0) TS(3, 1);
    const char* sA = smem + (c & 1) * K4_ST;
    const char* sB = sA + 16384;
    bf16x8 af[4], bf[4][3];
#pragma unroll
    for (int s = 0; s < 4; ++s) {
      af[s] = *(const bf16x8*)(sA + arow * 128 + (((2 * s + h) ^ ((arow >> 1) & 7)) << 4));
      const int k0 = 16 * s + 8 * h + q4;
#pragma unroll
      for (int j = 0; j < 3; ++j)
        bf[s][j] = cat8(tr_read(sB + k0 * 384 + ((nb + j * 64) ^ (((k0 >> 1) & 1) << 6))),
                        tr_read(sB + (k0 + 4) * 384 + ((nb + j * 64) ^ ((((k0 + 4) >> 1) & 1) << 6))));
    }
    if (c < 7) { K4_WRITE(c + 1); if (c + 1 + PD < 8) K4_LOAD(c + 1 + PD); }
#pragma unroll
    for (int s = 0; s < 4; ++s)
#pragma unroll
      for (int j = 0; j < 3; ++j)
        acc[j] = __builtin_amdgcn_mfma_f32_32x32x16_bf16(bf[s][j], af[s], acc[j], 0, 0, 0);
  }
  TS(3, 2);
  const float mean = sstat[arow * 2], rstd = sstat[arow * 2 + 1];
#pragma unroll
  for (int j = 0; j < 3; ++j) {
    if (wn * 3 + j < cnt) {
      const int fc = f0 + (wn * 3 + j) * 16 + 4 * h;
      const float* cp = scs + (wn * 3 + j) * 32 + 4 * h;
      const f32x4 g0 = *(const f32x4*)(cp), g1 = *(const f32x4*)(cp + 8);
      const f32x4 u0 = *(const f32x4*)(cp + 16), u1 = *(const f32x4*)(cp + 24);
      short* go = (short*)(ws + WS_G) + (size_t)(m0 + arow) * FF + fc;
      float r[8];
#pragma unroll
      for (int i = 0; i < 8; ++i) {
        const float gt = (acc[j][i] - mean * (i < 4 ? g0[i & 3] : g1[i & 3])) * rstd;
        const float up = (acc[j][i + 8] - mean * (i < 4 ? u0[i & 3] : u1[i & 3])) * rstd;
        r[i] = gt * __builtin_amdgcn_rcpf(1.f + __expf(-gt)) * up;
      }
      u32x2 a, c;
      a[0] = cvtpk(r[0], r[1]); a[1] = cvtpk(r[2], r[3]);
      c[0] = cvtpk(r[4], r[5]); c[1] = cvtpk(r[6], r[7]);
      *(u32x2*)go = a;
      *(u32x2*)(go + 8) = c;
    }
  }
  TS(3, 3);
#undef K4_LOAD
#undef K4_WRITE
}

extern "C" void kernel_launch(void* const* d_in, const int* in_sizes, int n_in, void* d_out, int out_size,
                              void* d_ws, size_t ws_size, hipStream_t stream) {
  const short* x = (const short*)d_in[0];
  const short* wq = (const short*)d_in[1];
  const short* wk = (const short*)d_in[2];
  const short* wv = (const short*)d_in[3];
  const short* wo = (const short*)d_in[4];
  const short* w1 = (const short*)d_in[6];
  const short* w3 = (const short*)d_in[7];
  const short* w2 = (const short*)d_in[8];
  char* ws = (char*)d_ws;
  (void)hipFuncSetAttribute((const void*)k1_ln_qkv, hipFuncAttributeMaxDynamicSharedMemorySize, K1_LDS);
  (void)hipFuncSetAttribute((const void*)k2_attn, hipFuncAttributeMaxDynamicSharedMemorySize, ATT_LDS);
  (void)hipFuncSetAttribute((const void*)k_gemm64<DM, true>, hipFuncAttributeMaxDynamicSharedMemorySize, KG_LDS);
  (void)hipFuncSetAttribute((const void*)k4_ffn1, hipFuncAttributeMaxDynamicSharedMemorySize, K4_LDS);
  (void)hipFuncSetAttribute((const void*)k_gemm64<FF, false>, hipFuncAttributeMaxDynamicSharedMemorySize, KG_LDS);
  k1_ln_qkv<<<256, 512, K1_LDS, stream>>>(x, wq, wk, wv, ws);
  k2_attn<<<256, 1024, ATT_LDS, stream>>>(w1, w3, wo, ws);
  k_gemm64<DM, true><<<256, 512, KG_LDS, stream>>>((const short*)(ws + WS_O), wo, x, (short*)(ws + WS_H), ws);
  k4_ffn1<<<256, 512, K4_LDS, stream>>>(w1, w3, ws);
  k_gemm64<FF, false><<<256, 512, KG_LDS, stream>>>((const short*)(ws + WS_G), w2, (const short*)(ws + WS_H), (short*)d_out, ws);
}
```

```cpp
#include <hip/hip_runtime.h>
#include <hip/hip_bf16.h>

typedef __attribute__((ext_vector_type(8))) short bf16x8;
typedef __attribute__((ext_vector_type(4))) short s16x4;
typedef __attribute__((ext_vector_type(16))) float f32x16;
typedef __attribute__((ext_vector_type(4))) float f32x4;
typedef __attribute__((ext_vector_type(2))) float f32x2;
typedef __attribute__((ext_vector_type(4))) unsigned u32x4;
typedef __attribute__((ext_vector_type(2))) unsigned u32x2;
typedef __attribute__((ext_vector_type(2))) __bf16 bf16x2_t;
typedef __attribute__((address_space(3))) s16x4 lds_s16x4;

#define DI __device__ __forceinline__

constexpr int S_LEN = 2048, DM = 512, NH = 32, DH = 16, FF = 1376;
constexpr float QSCALE = 0.25f * 1.4426950408889634f;

constexpr size_t WS_Q = 0;
constexpr size_t WS_KF = WS_Q + (size_t)S_LEN * DM * 2;
constexpr size_t WS_VF = WS_KF + (size_t)S_LEN * DH * 2;
constexpr size_t WS_O = WS_VF + (size_t)S_LEN * DH * 2;
constexpr size_t WS_H = WS_O + (size_t)S_LEN * DM * 2;
constexpr size_t WS_G = WS_H + (size_t)S_LEN * DM * 2;
constexpr size_t WS_KM = WS_G + (size_t)S_LEN * FF * 2;
constexpr size_t WS_ST = WS_KM + 512;
constexpr size_t WS_CS = WS_ST + (size_t)S_LEN * 32 * 4;
constexpr size_t WS_WOF = WS_CS + 172 * 128 * 4;
constexpr size_t WS_W13F = WS_WOF + 512 * 1024;
constexpr size_t WS_W2F = WS_W13F + 2752 * 1024;

DI unsigned cvtpk(float lo, float hi) {
  const f32x2 v = {lo, hi};
  return __builtin_bit_cast(unsigned, __builtin_convertvector(v, bf16x2_t));
}
DI float bf2f(short v) { return __uint_as_float(((unsigned)(unsigned short)v) << 16); }
DI float bflo(unsigned u) { return __uint_as_float(u << 16); }
DI float bfhi(unsigned u) { return __uint_as_float(u & 0xffff0000u); }
DI s16x4 tr_read(const char* p) {
  return __builtin_amdgcn_ds_read_tr16_b64_v4i16((lds_s16x4*)p);
}
DI bf16x8 cat8(s16x4 a, s16x4 b) { return __builtin_shufflevector(a, b, 0, 1, 2, 3, 4, 5, 6, 7); }
DI void stat2(unsigned u, float& s, float& q) {
  const float a = bflo(u), b = bfhi(u);
  s += a + b;
  q = fmaf(a, a, fmaf(b, b, q));
}
DI void stat8(const bf16x8& v, float& s, float& q) {
  const u32x4 u = __builtin_bit_cast(u32x4, v);
#pragma unroll
  for (int j = 0; j < 4; ++j) stat2(u[j], s, q);
}
typedef __attribute__((address_space(3))) void lvoid_t;
DI void glds16(const void* gsrc, unsigned lds_dst) {
  unsigned keep;
  asm volatile("s_mov_b32 %0, m0\n\ts_mov_b32 m0, %2\n\ts_nop 0\n\tglobal_load_lds_dwordx4 %1, off\n\ts_mov_b32 m0, %0"
               : "=&s"(keep) : "v"(gsrc), "s"(lds_dst) : "memory");
}
#define TS(kid, i)

constexpr int PD = 3;

constexpr int K1_ST = 36864;
constexpr int K1_LDS = 2 * K1_ST + 1024;
__global__ __launch_bounds__(512) void k1_ln_qkv(const short* __restrict__ x, const short* __restrict__ wq,
                                                 const short* __restrict__ wk, const short* __restrict__ wv,
                                                 char* __restrict__ ws) {
  extern __shared__ __attribute__((aligned(16))) char smem[];
  float* sstat = (float*)(smem + 2 * K1_ST);
  const int tid = threadIdx.x, lane = tid & 63, w = tid >> 6;
  const int b = blockIdx.x, xcd = b & 7, idx = b >> 3;
  const int mt = xcd * 4 + (idx & 3), nt = idx >> 2;
  const int m0 = mt * 64, n0 = nt * 64;
  const int ar = tid >> 4, ac = tid & 15, bk = tid >> 3, bc = tid & 7;
  const short* wkv = (nt & 1) ? wv : wk;
  TS(0, 0);
  bf16x8 av[PD][2], bv[PD][2], cv[PD];
#define K1_LOAD(c) do { _Pragma("unroll") for (int i = 0; i < 2; ++i) { \
      av[(c) % PD][i] = *(const bf16x8*)(x + (size_t)(m0 + i * 32 + ar) * DM + (c) * 128 + ac * 8); \
      bv[(c) % PD][i] = *(const bf16x8*)(wq + (size_t)((c) * 128 + i * 64 + bk) * DM + n0 + bc * 8); } \
    cv[(c) % PD] = *(const bf16x8*)(wkv + (size_t)(c) * 2048 + (tid & 255) * 8); } while (0)
#define K1_WRITE(c) do { char* sA_ = smem + ((c) & 1) * K1_ST; _Pragma("unroll") for (int i = 0; i < 2; ++i) { \
      const int row = i * 32 + ar; \
      *(bf16x8*)(sA_ + row * 256 + ((ac ^ (row & 15)) << 4)) = av[(c) % PD][i]; \
      stat8(av[(c) % PD][i], ssum[i], ssq[i]); \
      const int k = i * 64 + bk; \
      *(bf16x8*)(sA_ + 16384 + k * 128 + ((bc << 4) ^ (((k >> 1) & 1) << 6))) = bv[(c) % PD][i]; } \
    *(bf16x8*)(sA_ + 32768 + (tid & 255) * 16) = cv[(c) % PD]; } while (0)
  float ssum[2] = {0.f, 0.f}, ssq[2] = {0.f, 0.f};
  K1_LOAD(0); K1_LOAD(1); K1_LOAD(2);
  K1_WRITE(0); K1_LOAD(3);

  const int r32 = lane & 31, h = lane >> 5, g = lane >> 4, q4 = (lane & 15) >> 2, p4 = lane & 3;
  const int wq_ = w & 3, kp = w >> 2;
  const int wm = wq_ >> 1, wn = wq_ & 1;
  const int arow = wm * 32 + r32;
  const int nb = (wn * 32 + (g & 1) * 16 + 4 * p4) * 2;
  const int rb = (nt >> 1) * 16;
  bf16x8 ones;
#pragma unroll
  for (int j = 0; j < 8; ++j) ones[j] = (short)0x3F80;
  f32x16 acc = {}, cs = {};
  f32x4 c4 = {}, cs4 = {};
#pragma unroll
  for (int c = 0; c < 4; ++c) {
    __syncthreads();
    if (c == 0) TS(0, 1);
    const char* sA = smem + (c & 1) * K1_ST;
    const char* sB = sA + 16384;
    bf16x8 af[4], bf[4];
#pragma unroll
    for (int ss = 0; ss < 4; ++ss) {
      const int s = 2 * ss + kp;
      af[ss] = *(const bf16x8*)(sA + arow * 256 + (((2 * s + h) ^ (arow & 15)) << 4));
      const int k0 = 16 * s + 8 * h + q4;
      bf[ss] = cat8(tr_read(sB + k0 * 128 + (nb ^ (((k0 >> 1) & 1) << 6))),
                    tr_read(sB + (k0 + 4) * 128 + (nb ^ ((((k0 + 4) >> 1) & 1) << 6))));
    }
    if (c < 3) { K1_WRITE(c + 1); }
#pragma unroll
    for (int ss = 0; ss < 4; ++ss) {
      acc = __builtin_amdgcn_mfma_f32_32x32x16_bf16(bf[ss], af[ss], acc, 0, 0, 0);
      cs = __builtin_amdgcn_mfma_f32_32x32x16_bf16(bf[ss], ones, cs, 0, 0, 0);
    }
    if (w == 5) {
      const char* sC = sA + 32768;
      const int krow = rb + (lane & 15);
#pragma unroll
      for (int s = 0; s < 4; ++s) {
        const bf16x8 a16 = *(const bf16x8*)(sA + krow * 256 + (((4 * s + g) ^ (krow & 15)) << 4));
        const int k0 = 32 * s + 8 * g + q4;
        const bf16x8 b16 = cat8(tr_read(sC + k0 * 32 + 8 * p4), tr_read(sC + (k0 + 4) * 32 + 8 * p4));
        if (nt & 1) {
          c4 = __builtin_amdgcn_mfma_f32_16x16x32_bf16(a16, b16, c4, 0, 0, 0);
          cs4 = __builtin_amdgcn_mfma_f32_16x16x32_bf16(ones, b16, cs4, 0, 0, 0);
        } else {
          c4 = __builtin_amdgcn_mfma_f32_16x16x32_bf16(b16, a16, c4, 0, 0, 0);
          cs4 = __builtin_amdgcn_mfma_f32_16x16x32_bf16(b16, ones, cs4, 0, 0, 0);
        }
      }
    }
  }
  TS(0, 2);
#pragma unroll
  for (int i = 0; i < 2; ++i) {
    float s_ = ssum[i], q_ = ssq[i];
#pragma unroll
    for (int o = 1; o < 16; o <<= 1) { s_ += __shfl_xor(s_, o); q_ += __shfl_xor(q_, o); }
    if (ac == 0) {
      const float mean = s_ * (1.f / DM);
      const float var = fmaxf(q_ * (1.f / DM) - mean * mean, 0.f);
      sstat[(i * 32 + ar) * 2] = mean;
      sstat[(i * 32 + ar) * 2 + 1] = rsqrtf(var + 1e-5f);
    }
  }
  __syncthreads();
  const float mean = sstat[arow * 2], rstd = sstat[arow * 2 + 1];
#pragma unroll
  for (int i = 0; i < 16; ++i) acc[i] = fmaf(-mean, cs[i], acc[i]);
  float* red = (float*)smem + wq_ * 1024 + lane;
  if (kp == 1) {
#pragma unroll
    for (int i = 0; i < 16; ++i) red[i * 64] = acc[i];
  }
  if (w == 5) {
    float r[4];
    u32x2 o;
    if (nt & 1) {
#pragma unroll
      for (int i = 0; i < 4; ++i) {
        const int rr = rb + 4 * g + i;
        r[i] = (c4[i] - sstat[rr * 2] * cs4[i]) * sstat[rr * 2 + 1];
      }
      o[0] = cvtpk(r[0], r[1]); o[1] = cvtpk(r[2], r[3]);
      const int key0 = m0 + rb;
      const int t = key0 >> 5, s = (key0 >> 4) & 1;
      short* vf = (short*)(ws + WS_VF) + (size_t)t * 512 + s * 256 + (g & 1) * 128 + (lane & 15) * 8 + (g >> 1) * 4;
      *(u32x2*)vf = o;
    } else {
      const int rr = rb + (lane & 15);
      const float mn = sstat[rr * 2], rs = sstat[rr * 2 + 1];
#pragma unroll
      for (int i = 0; i < 4; ++i) r[i] = (c4[i] - mn * cs4[i]) * rs;
      o[0] = cvtpk(r[0], r[1]); o[1] = cvtpk(r[2], r[3]);
      const int key = m0 + rr;
      const int t = key >> 5;
      short* kf = (short*)(ws + WS_KF) + (size_t)t * 512 + (g >> 1) * 256 + (key & 31) * 8 + (g & 1) * 4;
      *(u32x2*)kf = o;
      const float k0 = bflo(o[0]), k1 = bfhi(o[0]), k2 = bflo(o[1]), k3 = bfhi(o[1]);
      float a = k0 * k0 + k1 * k1 + k2 * k2 + k3 * k3;
      a += __shfl_xor(a, 16);
      a += __shfl_xor(a, 32);
#pragma unroll
      for (int of = 8; of > 0; of >>= 1) a = fmaxf(a, __shfl_xor(a, of));
      if (lane == 0) ((float*)(ws + WS_KM))[mt * 4 + (nt >> 1)] = a;
    }
  }
  __syncthreads();
  if (kp == 1) return;
  const float sc = rstd * QSCALE;
  short* qo = (short*)(ws + WS_Q) + (size_t)(m0 + arow) * DM + n0 + wn * 32 + 4 * h;
#pragma unroll
  for (int gg = 0; gg < 4; ++gg) {
    u32x2 o;
    o[0] = cvtpk((acc[4 * gg] + red[(4 * gg) * 64]) * sc, (acc[4 * gg + 1] + red[(4 * gg + 1) * 64]) * sc);
    o[1] = cvtpk((acc[4 * gg + 2] + red[(4 * gg + 2) * 64]) * sc, (acc[4 * gg + 3] + red[(4 * gg + 3) * 64]) * sc);
    *(u32x2*)(qo + 8 * gg) = o;
  }
  TS(0, 3);
#undef K1_LOAD
#undef K1_WRITE
}

constexpr int TS = 2080;
constexpr int ATT_RED = 64 * TS;
constexpr int ATT_LDS = ATT_RED + 8 * 64 * 9 * 4 + 1024;

__global__ __launch_bounds__(1024) void k2_attn(const short* __restrict__ w1, const short* __restrict__ w3,
                                                const short* __restrict__ wo, const short* __restrict__ w2,
                                                char* __restrict__ ws) {
  extern __shared__ __attribute__((aligned(16))) char smem[];
  const int tid = threadIdx.x, lane = tid & 63, w = tid >> 6;
  const int r32 = lane & 31, h = lane >> 5;
  float* scomb = (float*)(smem + ATT_RED);
  TS(1, 0);
  const int wr = w & 7, kh = w >> 3;
  const int pblk = (blockIdx.x & 7) * 32 + (blockIdx.x >> 3);
  const size_t row = (size_t)pblk * 256 + wr * 32 + r32;
  const int tl0 = (w < 8) ? w : 24 + w;
  bf16x8 kv[8];
  {
    const bf16x8* srck = (const bf16x8*)(ws + WS_KF) + tl0 * 64 + lane;
    const bf16x8* srcv = (const bf16x8*)(ws + WS_VF) + tl0 * 64 + lane;
#pragma unroll
    for (int j = 0; j < 4; ++j) { kv[2 * j] = srck[j * 512]; kv[2 * j + 1] = srcv[j * 512]; }
  }
  const bf16x8 qf = *(const bf16x8*)((const short*)(ws + WS_Q) + row * 16 + h * 8);
  float kn2 = fmaxf(((const float*)(ws + WS_KM))[lane], ((const float*)(ws + WS_KM))[64 + lane]);
  bf16x8 csv0, csv1, rl;
  if (tid < 512) *(unsigned*)(smem + (tid >> 3) * TS + 2048 + (tid & 7) * 4) = (tid & 7) < 4 ? 0u : 0x3F803F80u;
  char* sdst = smem + tl0 * TS + lane * 16;
  *(bf16x8*)(sdst) = kv[0];
  *(bf16x8*)(sdst + 1024) = kv[1];
#pragma unroll
  for (int o = 32; o > 0; o >>= 1) kn2 = fmaxf(kn2, __shfl_xor(kn2, o));
  float qn2 = 0.f;
#pragma unroll
  for (int j = 0; j < 8; ++j) { const float f = bf2f(qf[j]); qn2 += f * f; }
  qn2 += __shfl_xor(qn2, 32);
  const float mref = sqrtf(qn2 * kn2) * 1.001f;

  const bool vlane = r32 < 16;
  const int kbase = lane * 16;
  const int vbase0 = vlane ? (1024 + h * 256 + r32 * 16) : (2048 + (r32 == 16 ? 16 : 0));
  const int vbase1 = vlane ? vbase0 + 512 : vbase0;
  f32x16 o = {};
  {
    f32x16 negm;
#pragma unroll
    for (int i = 0; i < 16; ++i) negm[i] = -mref;
    const char* kp = smem + kbase + kh * 32 * TS;
    const char* vp0 = smem + vbase0 + kh * 32 * TS;
    const char* vp1 = smem + vbase1 + kh * 32 * TS;
#pragma unroll
    for (int j = 0; j < 4; ++j) {
      __syncthreads();
      TS(1, 1 + j);
      if (j == 0) {
  {
    const int gi = blockIdx.x < 172 ? blockIdx.x : 0, t2 = tid & 511;
    const short* wsrc = ((gi < 86) ? (w1 + gi * 16) : (w3 + (gi - 86) * 16)) + (size_t)(t2 >> 1) * FF + (t2 & 1) * 8;
    csv0 = *(const bf16x8*)(wsrc);
    csv1 = *(const bf16x8*)(wsrc + (size_t)256 * FF);
  }
      }
      if (j < 3) {
        *(bf16x8*)(sdst + (j + 1) * 8 * TS) = kv[2 * j + 2];
        *(bf16x8*)(sdst + (j + 1) * 8 * TS + 1024) = kv[2 * j + 3];
      }
      if (j >= 1 && j <= 2) {
        const int u = blockIdx.x * 16 + w + (j - 1) * 4096;
        if (u < 4640) {
          char* scr = (char*)scomb + w * 1024;
          *(bf16x8*)(scr + (lane >> 2) * 64 + (lane & 3) * 16) = rl;
          const int g4 = lane >> 4;
          const char* rp = scr + (8 * (g4 >> 1) + ((lane & 15) >> 2)) * 64 + ((g4 & 1) * 16 + 4 * (lane & 3)) * 2;
          char* dst = ws + WS_WOF + (size_t)u * 1024;
          *(bf16x8*)(dst + lane * 16) = cat8(tr_read(rp), tr_read(rp + 256));
        }
      }
      if (j <= 1) {
        int u = blockIdx.x * 16 + w + j * 4096;
        u = u < 4640 ? u : 0;
        const int kk = lane >> 2, pc = lane & 3;
        const short* src;
        if (u < 512) {
          src = wo + (size_t)(16 * (u & 31) + kk) * DM + 32 * (u >> 5) + pc * 8;
        } else if (u < 3264) {
          const int v = u - 512;
          src = (pc < 2 ? w1 : w3) + (size_t)(16 * (v & 31) + kk) * FF + (v >> 5) * 16 + (pc & 1) * 8;
        } else {
          const int v = u - 3264;
          src = w2 + (size_t)(16 * (v % 86) + kk) * DM + 32 * (v / 86) + pc * 8;
        }
        rl = *(const bf16x8*)src;
      }
#pragma unroll
      for (int t = j * 8; t < j * 8 + 8; ++t) {
        const bf16x8 kf = *(const bf16x8*)(kp + t * TS);
        const bf16x8 vf0 = *(const bf16x8*)(vp0 + t * TS);
        const bf16x8 vf1 = *(const bf16x8*)(vp1 + t * TS);
        const f32x16 s = __builtin_amdgcn_mfma_f32_32x32x16_bf16(kf, qf, negm, 0, 0, 0);
        u32x4 p0, p1;
#pragma unroll
        for (int i = 0; i < 4; ++i) {
          p0[i] = cvtpk(__builtin_amdgcn_exp2f(s[2 * i]), __builtin_amdgcn_exp2f(s[2 * i + 1]));
          p1[i] = cvtpk(__builtin_amdgcn_exp2f(s[8 + 2 * i]), __builtin_amdgcn_exp2f(s[8 + 2 * i + 1]));
        }
        o = __builtin_amdgcn_mfma_f32_32x32x16_bf16(vf0, __builtin_bit_cast(bf16x8, p0), o, 0, 0, 0);
        o = __builtin_amdgcn_mfma_f32_32x32x16_bf16(vf1, __builtin_bit_cast(bf16x8, p1), o, 0, 0, 0);
      }
    }
  }
  TS(1, 5);
  TS(1, 6);
  float* cb = scomb + wr * (64 * 9) + lane * 9;
  if (kh == 1) {
#pragma unroll
    for (int i = 0; i < 9; ++i) cb[i] = o[i];
  }
  __syncthreads();
  if (kh == 1) {
    if (blockIdx.x < 172) {
      float cf[8];
#pragma unroll
      for (int j = 0; j < 8; ++j) {
        cf[j] = bf2f(csv0[j]) + bf2f(csv1[j]);
#pragma unroll
        for (int of = 2; of < 64; of <<= 1) cf[j] += __shfl_xor(cf[j], of);
      }
      if (lane < 2) {
        float* dst = (float*)(ws + WS_CS) + (size_t)(blockIdx.x * 8 + wr) * 16 + lane * 8;
        *(f32x4*)dst = f32x4{cf[0], cf[1], cf[2], cf[3]};
        *(f32x4*)(dst + 4) = f32x4{cf[4], cf[5], cf[6], cf[7]};
      }
    }
    return;
  }
#pragma unroll
  for (int i = 0; i < 9; ++i) o[i] += cb[i];
  float l = __shfl(o[8], r32);
  if (__any(l < 1e-18f)) {
    f32x16 negm = {};
    o = f32x16{};
#pragma unroll 1
    for (int t = 0; t < 64; ++t) {
      const bf16x8 kf = *(const bf16x8*)(smem + kbase + t * TS);
      const bf16x8 vf0 = *(const bf16x8*)(smem + vbase0 + t * TS);
      const bf16x8 vf1 = *(const bf16x8*)(smem + vbase1 + t * TS);
      f32x16 s = __builtin_amdgcn_mfma_f32_32x32x16_bf16(kf, qf, negm, 0, 0, 0);
      float mx = s[0];
#pragma unroll
      for (int i = 1; i < 16; ++i) mx = fmaxf(mx, s[i]);
      float d = fmaxf(mx, __shfl_xor(mx, 32));
      if (t != 0) d = fmaxf(d, 0.f);
      const float sc = (t != 0) ? __builtin_amdgcn_exp2f(-d) : 1.f;
#pragma unroll
      for (int i = 0; i < 16; ++i) { s[i] -= d; negm[i] -= d; o[i] *= sc; }
      u32x4 p0, p1;
#pragma unroll
      for (int i = 0; i < 4; ++i) {
        p0[i] = cvtpk(__builtin_amdgcn_exp2f(s[2 * i]), __builtin_amdgcn_exp2f(s[2 * i + 1]));
        p1[i] = cvtpk(__builtin_amdgcn_exp2f(s[8 + 2 * i]), __builtin_amdgcn_exp2f(s[8 + 2 * i + 1]));
      }
      o = __builtin_amdgcn_mfma_f32_32x32x16_bf16(vf0, __builtin_bit_cast(bf16x8, p0), o, 0, 0, 0);
      o = __builtin_amdgcn_mfma_f32_32x32x16_bf16(vf1, __builtin_bit_cast(bf16x8, p1), o, 0, 0, 0);
    }
    l = __shfl(o[8], r32);
  }
  const float inv = 1.f / l;
  short* op = (short*)(ws + WS_O) + row * 16 + 4 * h;
  u32x2 a, c;
  a[0] = cvtpk(o[0] * inv, o[1] * inv); a[1] = cvtpk(o[2] * inv, o[3] * inv);
  c[0] = cvtpk(o[4] * inv, o[5] * inv); c[1] = cvtpk(o[6] * inv, o[7] * inv);
  *(u32x2*)op = a;
  *(u32x2*)(op + 8) = c;
  TS(1, 7);
}

constexpr int KG_LDS = 3 * 32768;
template <int KTOT, bool STATS>
__global__ __launch_bounds__(512) void k_gemm64(const short* __restrict__ A, const char* __restrict__ BF,
                                                const short* __restrict__ resid, short* __restrict__ out,
                                                char* __restrict__ ws) {
  extern __shared__ __attribute__((aligned(16))) char smem[];
  constexpr int NCH = (KTOT + 127) / 128;
  constexpr int KLAST = KTOT - (NCH - 1) * 128;
  constexpr int NKS = KTOT / 16;
  const int tid = threadIdx.x, lane = tid & 63, w = __builtin_amdgcn_readfirstlane(tid >> 6);
  const int b = blockIdx.x, xcd = b & 7, idx = b >> 3;
  const int mt = xcd * 4 + (idx & 3), nt = idx >> 2;
  const int m0 = mt * 64, n0 = nt * 64;
  TS((STATS ? 2 : 4), 0);
  const short* asrc[2];
  const char* bsrc[2];
  int aco[2];
#pragma unroll
  for (int i = 0; i < 2; ++i) {
    const int row = (w * 2 + i) * 4 + (lane >> 4);
    aco[i] = ((lane & 15) ^ (row & 15)) << 3;
    asrc[i] = A + (size_t)(m0 + row) * KTOT;
    const int e = w * 2 + i;
    bsrc[i] = BF + (size_t)((nt * 2 + (e >> 3)) * NKS + (e & 7)) * 1024 + lane * 16;
  }
  const unsigned lbase = (unsigned)(size_t)(lvoid_t*)smem;
#define KG_DMA(c) do { const unsigned st_ = lbase + ((c) % 3) * 32768; _Pragma("unroll") for (int i = 0; i < 2; ++i) { \
      const int ka = (((c) < NCH - 1) || (aco[i] < KLAST)) ? (c) * 128 + aco[i] : 0; \
      glds16(asrc[i] + ka, st_ + (w * 2 + i) * 1024); \
      const int kb = (((c) < NCH - 1) || (((w * 2 + i) & 7) * 16 < KLAST)) ? (c) * 8192 : 0; \
      glds16(bsrc[i] + kb, st_ + 16384 + (w * 2 + i) * 1024); } } while (0)
  KG_DMA(0); KG_DMA(1);

  const int r32 = lane & 31, h = lane >> 5;
  const int wq_ = w & 3, kp = w >> 2;
  const int wm = wq_ >> 1, wn = wq_ & 1;
  const int arow = wm * 32 + r32;
  const size_t off = (size_t)(m0 + arow) * DM + n0 + wn * 32 + 4 * h;
  s16x4 xr[4];
#pragma unroll
  for (int gg = 0; gg < 4; ++gg) xr[gg] = *(const s16x4*)(resid + off + 8 * gg);
  asm volatile("" :: "v"(xr[0]), "v"(xr[1]), "v"(xr[2]), "v"(xr[3]));
  f32x16 acc = {};
#pragma unroll
  for (int c = 0; c < NCH; ++c) {
    if (c < NCH - 1) asm volatile("s_waitcnt vmcnt(4)" ::: "memory");
    else             asm volatile("s_waitcnt vmcnt(0)" ::: "memory");
    asm volatile("s_waitcnt lgkmcnt(0)" ::: "memory");
    __builtin_amdgcn_s_barrier();
    if (c == 0) TS((STATS ? 2 : 4), 1);
    if (c + 2 < NCH) KG_DMA(c + 2);
    const char* sA = smem + (c % 3) * 32768;
    const char* sB = sA + 16384;
#pragma unroll
    for (int ss = 0; ss < 4; ++ss) {
      const int s = 2 * ss + kp;
      if (c < NCH - 1 || 16 * (2 * ss) < KLAST) {
        const bf16x8 af = *(const bf16x8*)(sA + arow * 256 + (((2 * s + h) ^ (arow & 15)) << 4));
        const bf16x8 bf = *(const bf16x8*)(sB + (wn * 8 + s) * 1024 + lane * 16);
        acc = __builtin_amdgcn_mfma_f32_32x32x16_bf16(bf, af, acc, 0, 0, 0);
      }
    }
  }
  TS((STATS ? 2 : 4), 2);
  float* red = (float*)(smem + (NCH % 3) * 32768) + wq_ * 1024 + lane;
  if (kp == 1) {
#pragma unroll
    for (int i = 0; i < 16; ++i) red[i * 64] = acc[i];
  }
  __syncthreads();
  if (kp == 1) return;
  float ssum = 0.f, ssq = 0.f;
#pragma unroll
  for (int gg = 0; gg < 4; ++gg) {
    u32x2 o;
    o[0] = cvtpk(acc[4 * gg] + red[(4 * gg) * 64] + bf2f(xr[gg][0]), acc[4 * gg + 1] + red[(4 * gg + 1) * 64] + bf2f(xr[gg][1]));
    o[1] = cvtpk(acc[4 * gg + 2] + red[(4 * gg + 2) * 64] + bf2f(xr[gg][2]), acc[4 * gg + 3] + red[(4 * gg + 3) * 64] + bf2f(xr[gg][3]));
    *(u32x2*)(out + off + 8 * gg) = o;
    if (STATS) {
      stat2(o[0], ssum, ssq);
      stat2(o[1], ssum, ssq);
    }
  }
  if (STATS) {
    ssum += __shfl_xor(ssum, 32);
    ssq += __shfl_xor(ssq, 32);
    if (h == 0) {
      f32x2 st = {ssum, ssq};
      *(f32x2*)((float*)(ws + WS_ST) + (size_t)(m0 + arow) * 32 + (nt * 2 + wn) * 2) = st;
    }
  }
  TS((STATS ? 2 : 4), 3);
#undef KG_DMA
}

constexpr int K4_ST = 40960;
constexpr int K4_LDS = 3 * K4_ST + 1024 + 1024;
__global__ __launch_bounds__(512) void k4_ffn1(char* __restrict__ ws) {
  extern __shared__ __attribute__((aligned(16))) char smem[];
  float* sstat = (float*)(smem + 3 * K4_ST);
  float* scs = sstat + 256;
  const int tid = threadIdx.x, lane = tid & 63, wall = __builtin_amdgcn_readfirstlane(tid >> 6);
  const int w = wall & 3;
  const bool loader = wall >= 4;
  const int b = blockIdx.x, xcd = b & 7, idx = b >> 3;
  const int mt = xcd * 2 + (idx & 1), ng = idx >> 1;
  const int m0 = mt * 128;
  const int cnt = ng < 6 ? 6 : 5;
  const int f0 = (ng * 5 + (ng < 6 ? ng : 6)) * 16;
  const short* H = (const short*)(ws + WS_H);
  TS(3, 0);
  const short* asrc[4];
  const short* bsrc[6];
#pragma unroll
  for (int i = 0; i < 4; ++i) {
    const int row = (w * 4 + i) * 8 + (lane >> 3);
    asrc[i] = H + (size_t)(m0 + row) * DM + (((lane & 7) ^ ((row >> 1) & 7)) << 3);
  }
#pragma unroll
  for (int i = 0; i < 6; ++i) {
    const int e = w * 6 + i, j = e >> 2, ks = e & 3;
    const int ct = (f0 >> 4) + (j < cnt ? j : 0);
    bsrc[i] = (const short*)(ws + WS_W13F + (size_t)(ct * 32 + ks) * 1024 + lane * 16);
  }
  const unsigned lbase = (unsigned)(size_t)(lvoid_t*)smem;
#define K4_DMA(c) do { const unsigned st_ = lbase + ((c) % 3) * K4_ST; \
    _Pragma("unroll") for (int i = 0; i < 4; ++i) glds16(asrc[i] + (c) * 64, st_ + (w * 4 + i) * 1024); \
    _Pragma("unroll") for (int i = 0; i < 6; ++i) glds16(bsrc[i] + (c) * 2048, st_ + 16384 + (w * 6 + i) * 1024); } while (0)
  if (loader) { K4_DMA(0); K4_DMA(1); }
  if (tid < 128) {
    const f32x4* sp = (const f32x4*)((const float*)(ws + WS_ST) + (size_t)(m0 + tid) * 32);
    float s_ = 0.f, q_ = 0.f;
#pragma unroll
    for (int i = 0; i < 8; ++i) { const f32x4 v = sp[i]; s_ += v[0] + v[2]; q_ += v[1] + v[3]; }
    const float mean = s_ * (1.f / DM);
    const float var = fmaxf(q_ * (1.f / DM) - mean * mean, 0.f);
    sstat[tid * 2] = mean;
    sstat[tid * 2 + 1] = rsqrtf(var + 1e-5f);
  }
  if (tid < 192) {
    const int e = tid, ct = e >> 5, isup = (e >> 4) & 1;
    const int gi = isup * 86 + (f0 >> 4) + (ct < cnt ? ct : 0);
    const float* cp = (const float*)(ws + WS_CS) + (size_t)gi * 128 + (e & 15);
    float a = 0.f;
#pragma unroll
    for (int i = 0; i < 8; ++i) a += cp[i * 16];
    scs[e] = a;
  }

  const int r32 = lane & 31, h = lane >> 5, g = lane >> 4, q4 = (lane & 15) >> 2, p4 = lane & 3;
  const int wm = w >> 1, wn = w & 1;
  if (loader) {
#pragma unroll
    for (int c = 0; c < 8; ++c) {
      if (c < 7) asm volatile("s_waitcnt vmcnt(10)" ::: "memory");
      else       asm volatile("s_waitcnt vmcnt(0)" ::: "memory");
      __builtin_amdgcn_s_barrier();
      if (c + 2 < 8) K4_DMA(c + 2);
    }
    return;
  }
  f32x16 acc[2][3] = {};
#pragma unroll
  for (int c = 0; c < 8; ++c) {
    asm volatile("s_waitcnt lgkmcnt(0)" ::: "memory");
    __builtin_amdgcn_s_barrier();
    if (c == 0) TS(3, 1);
    const char* sA = smem + (c % 3) * K4_ST;
    const char* sB = sA + 16384;
#pragma unroll
    for (int s = 0; s < 4; ++s) {
      bf16x8 af[2], bf[3];
#pragma unroll
      for (int i = 0; i < 2; ++i) {
        const int arow = wm * 64 + i * 32 + r32;
        af[i] = *(const bf16x8*)(sA + arow * 128 + (((2 * s + h) ^ ((arow >> 1) & 7)) << 4));
      }
#pragma unroll
      for (int j = 0; j < 3; ++j) bf[j] = *(const bf16x8*)(sB + ((wn * 3 + j) * 4 + s) * 1024 + lane * 16);
#pragma unroll
      for (int i = 0; i < 2; ++i)
#pragma unroll
        for (int j = 0; j < 3; ++j)
          acc[i][j] = __builtin_amdgcn_mfma_f32_32x32x16_bf16(bf[j], af[i], acc[i][j], 0, 0, 0);
    }
  }
  TS(3, 2);
#pragma unroll
  for (int i2 = 0; i2 < 2; ++i2) {
    const int arow = wm * 64 + i2 * 32 + r32;
    const float mean = sstat[arow * 2], rstd = sstat[arow * 2 + 1];
#pragma unroll
    for (int j = 0; j < 3; ++j) {
      if (wn * 3 + j < cnt) {
        const int fc = f0 + (wn * 3 + j) * 16 + 4 * h;
        const float* cp = scs + (wn * 3 + j) * 32 + 4 * h;
        const f32x4 g0 = *(const f32x4*)(cp), g1 = *(const f32x4*)(cp + 8);
        const f32x4 u0 = *(const f32x4*)(cp + 16), u1 = *(const f32x4*)(cp + 24);
        short* go = (short*)(ws + WS_G) + (size_t)(m0 + arow) * FF + fc;
        float r[8];
#pragma unroll
        for (int i = 0; i < 8; ++i) {
          const float gt = (acc[i2][j][i] - mean * (i < 4 ? g0[i & 3] : g1[i & 3])) * rstd;
          const float up = (acc[i2][j][i + 8] - mean * (i < 4 ? u0[i & 3] : u1[i & 3])) * rstd;
          r[i] = gt * __builtin_amdgcn_rcpf(1.f + __expf(-gt)) * up;
        }
        u32x2 a, c;
        a[0] = cvtpk(r[0], r[1]); a[1] = cvtpk(r[2], r[3]);
        c[0] = cvtpk(r[4], r[5]); c[1] = cvtpk(r[6], r[7]);
        *(u32x2*)go = a;
        *(u32x2*)(go + 8) = c;
      }
    }
  }
  TS(3, 3);
#undef K4_DMA
}

extern "C" void kernel_launch(void* const* d_in, const int* in_sizes, int n_in, void* d_out, int out_size,
                              void* d_ws, size_t ws_size, hipStream_t stream) {
  const short* x = (const short*)d_in[0];
  const short* wq = (const short*)d_in[1];
  const short* wk = (const short*)d_in[2];
  const short* wv = (const short*)d_in[3];
  const short* wo = (const short*)d_in[4];
  const short* w1 = (const short*)d_in[6];
  const short* w3 = (const short*)d_in[7];
  const short* w2 = (const short*)d_in[8];
  char* ws = (char*)d_ws;
  (void)hipFuncSetAttribute((const void*)k1_ln_qkv, hipFuncAttributeMaxDynamicSharedMemorySize, K1_LDS);
  (void)hipFuncSetAttribute((const void*)k2_attn, hipFuncAttributeMaxDynamicSharedMemorySize, ATT_LDS);
  (void)hipFuncSetAttribute((const void*)k_gemm64<DM, true>, hipFuncAttributeMaxDynamicSharedMemorySize, KG_LDS);
  (void)hipFuncSetAttribute((const void*)k4_ffn1, hipFuncAttributeMaxDynamicSharedMemorySize, K4_LDS);
  (void)hipFuncSetAttribute((const void*)k_gemm64<FF, false>, hipFuncAttributeMaxDynamicSharedMemorySize, KG_LDS);
  k1_ln_qkv<<<256, 512, K1_LDS, stream>>>(x, wq, wk, wv, ws);
  k2_attn<<<256, 1024, ATT_LDS, stream>>>(w1, w3, wo, w2, ws);
  k_gemm64<DM, true><<<256, 512, KG_LDS, stream>>>((const short*)(ws + WS_O), ws + WS_WOF, x, (short*)(ws + WS_H), ws);
  k4_ffn1<<<256, 512, K4_LDS, stream>>>(ws);
  k_gemm64<FF, false><<<256, 512, KG_LDS, stream>>>((const short*)(ws + WS_G), ws + WS_W2F, (const short*)(ws + WS_H), (short*)d_out, ws);
}
```

```cpp
#include <hip/hip_runtime.h>
#include <hip/hip_bf16.h>

typedef __attribute__((ext_vector_type(8))) short bf16x8;
typedef __attribute__((ext_vector_type(4))) short s16x4;
typedef __attribute__((ext_vector_type(16))) float f32x16;
typedef __attribute__((ext_vector_type(4))) float f32x4;
typedef __attribute__((ext_vector_type(2))) float f32x2;
typedef __attribute__((ext_vector_type(4))) unsigned u32x4;
typedef __attribute__((ext_vector_type(2))) unsigned u32x2;
typedef __attribute__((ext_vector_type(2))) __bf16 bf16x2_t;
typedef __attribute__((address_space(3))) s16x4 lds_s16x4;

#define DI __device__ __forceinline__

constexpr int S_LEN = 2048, DM = 512, NH = 32, DH = 16, FF = 1376;
constexpr float QSCALE = 0.25f * 1.4426950408889634f;

constexpr size_t WS_Q = 0;
constexpr size_t WS_KF = WS_Q + (size_t)S_LEN * DM * 2;
constexpr size_t WS_VF = WS_KF + (size_t)S_LEN * DH * 2;
constexpr size_t WS_O = WS_VF + (size_t)S_LEN * DH * 2;
constexpr size_t WS_H = WS_O + (size_t)S_LEN * DM * 2;
constexpr size_t WS_G = WS_H + (size_t)S_LEN * DM * 2;
constexpr size_t WS_KM = WS_G + (size_t)S_LEN * FF * 2;
constexpr size_t WS_ST = WS_KM + 512;
constexpr size_t WS_CS = WS_ST + (size_t)S_LEN * 32 * 4;
constexpr size_t WS_WOF = WS_CS + 172 * 128 * 4;
constexpr size_t WS_W13F = WS_WOF + 512 * 1024;
constexpr size_t WS_W2F = WS_W13F + 2752 * 1024;

DI unsigned cvtpk(float lo, float hi) {
  const f32x2 v = {lo, hi};
  return __builtin_bit_cast(unsigned, __builtin_convertvector(v, bf16x2_t));
}
DI float bf2f(short v) { return __uint_as_float(((unsigned)(unsigned short)v) << 16); }
DI float bflo(unsigned u) { return __uint_as_float(u << 16); }
DI float bfhi(unsigned u) { return __uint_as_float(u & 0xffff0000u); }
DI s16x4 tr_read(const char* p) {
  return __builtin_amdgcn_ds_read_tr16_b64_v4i16((lds_s16x4*)p);
}
DI bf16x8 cat8(s16x4 a, s16x4 b) { return __builtin_shufflevector(a, b, 0, 1, 2, 3, 4, 5, 6, 7); }
DI void stat2(unsigned u, float& s, float& q) {
  const float a = bflo(u), b = bfhi(u);
  s += a + b;
  q = fmaf(a, a, fmaf(b, b, q));
}
DI void stat8(const bf16x8& v, float& s, float& q) {
  const u32x4 u = __builtin_bit_cast(u32x4, v);
#pragma unroll
  for (int j = 0; j < 4; ++j) stat2(u[j], s, q);
}
typedef __attribute__((address_space(3))) void lvoid_t;
DI void glds16(const void* gsrc, unsigned lds_dst) {
  unsigned keep;
  asm volatile("s_mov_b32 %0, m0\n\ts_mov_b32 m0, %2\n\ts_nop 0\n\tglobal_load_lds_dwordx4 %1, off\n\ts_mov_b32 m0, %0"
               : "=&s"(keep) : "v"(gsrc), "s"(lds_dst) : "memory");
}
DI void st16_wt(void* p, u32x4 v) {
  asm volatile("global_store_dwordx4 %0, %1, off sc1\n\ts_nop 1" :: "v"(p), "v"(v) : "memory");
}
DI u32x4 widen16(u32x2 a, u32x2 b) {
  const auto r0 = __builtin_amdgcn_permlane32_swap(a[0], b[0], false, false);
  const auto r1 = __builtin_amdgcn_permlane32_swap(a[1], b[1], false, false);
  return u32x4{r0[0], r1[0], r0[1], r1[1]};
}
#define TS(kid, i)

constexpr int PD = 3;

constexpr int K1_ST = 36864;
constexpr int K1_LDS = 2 * K1_ST + 1024;
__global__ __launch_bounds__(512) void k1_ln_qkv(const short* __restrict__ x, const short* __restrict__ wq,
                                                 const short* __restrict__ wk, const short* __restrict__ wv,
                                                 char* __restrict__ ws) {
  extern __shared__ __attribute__((aligned(16))) char smem[];
  float* sstat = (float*)(smem + 2 * K1_ST);
  const int tid = threadIdx.x, lane = tid & 63, w = tid >> 6;
  const int b = blockIdx.x, xcd = b & 7, idx = b >> 3;
  const int mt = xcd * 4 + (idx & 3), nt = idx >> 2;
  const int m0 = mt * 64, n0 = nt * 64;
  const int ar = tid >> 4, ac = tid & 15, bk = tid >> 3, bc = tid & 7;
  const short* wkv = (nt & 1) ? wv : wk;
  TS(0, 0);
  bf16x8 av[PD][2], bv[PD][2], cv[PD];
#define K1_LOAD(c) do { _Pragma("unroll") for (int i = 0; i < 2; ++i) { \
      av[(c) % PD][i] = *(const bf16x8*)(x + (size_t)(m0 + i * 32 + ar) * DM + (c) * 128 + ac * 8); \
      bv[(c) % PD][i] = *(const bf16x8*)(wq + (size_t)((c) * 128 + i * 64 + bk) * DM + n0 + bc * 8); } \
    cv[(c) % PD] = *(const bf16x8*)(wkv + (size_t)(c) * 2048 + (tid & 255) * 8); } while (0)
#define K1_WRITE(c) do { char* sA_ = smem + ((c) & 1) * K1_ST; _Pragma("unroll") for (int i = 0; i < 2; ++i) { \
      const int row = i * 32 + ar; \
      *(bf16x8*)(sA_ + row * 256 + ((ac ^ (row & 15)) << 4)) = av[(c) % PD][i]; \
      stat8(av[(c) % PD][i], ssum[i], ssq[i]); \
      const int k = i * 64 + bk; \
      *(bf16x8*)(sA_ + 16384 + k * 128 + ((bc << 4) ^ (((k >> 1) & 1) << 6))) = bv[(c) % PD][i]; } \
    *(bf16x8*)(sA_ + 32768 + (tid & 255) * 16) = cv[(c) % PD]; } while (0)
  float ssum[2] = {0.f, 0.f}, ssq[2] = {0.f, 0.f};
  K1_LOAD(0); K1_LOAD(1); K1_LOAD(2);
  K1_WRITE(0); K1_LOAD(3);

  const int r32 = lane & 31, h = lane >> 5, g = lane >> 4, q4 = (lane & 15) >> 2, p4 = lane & 3;
  const int wq_ = w & 3, kp = w >> 2;
  const int wm = wq_ >> 1, wn = wq_ & 1;
  const int arow = wm * 32 + r32;
  const int nb = (wn * 32 + (g & 1) * 16 + 4 * p4) * 2;
  const int rb = (nt >> 1) * 16;
  bf16x8 ones;
#pragma unroll
  for (int j = 0; j < 8; ++j) ones[j] = (short)0x3F80;
  f32x16 acc = {}, cs = {};
  f32x4 c4 = {}, cs4 = {};
#pragma unroll
  for (int c = 0; c < 4; ++c) {
    __syncthreads();
    if (c == 0) TS(0, 1);
    const char* sA = smem + (c & 1) * K1_ST;
    const char* sB = sA + 16384;
    bf16x8 af[4], bf[4];
#pragma unroll
    for (int ss = 0; ss < 4; ++ss) {
      const int s = 2 * ss + kp;
      af[ss] = *(const bf16x8*)(sA + arow * 256 + (((2 * s + h) ^ (arow & 15)) << 4));
      const int k0 = 16 * s + 8 * h + q4;
      bf[ss] = cat8(tr_read(sB + k0 * 128 + (nb ^ (((k0 >> 1) & 1) << 6))),
                    tr_read(sB + (k0 + 4) * 128 + (nb ^ ((((k0 + 4) >> 1) & 1) << 6))));
    }
    if (c < 3) { K1_WRITE(c + 1); }
#pragma unroll
    for (int ss = 0; ss < 4; ++ss) {
      acc = __builtin_amdgcn_mfma_f32_32x32x16_bf16(bf[ss], af[ss], acc, 0, 0, 0);
      cs = __builtin_amdgcn_mfma_f32_32x32x16_bf16(bf[ss], ones, cs, 0, 0, 0);
    }
    if (w == 5) {
      const char* sC = sA + 32768;
      const int krow = rb + (lane & 15);
#pragma unroll
      for (int s = 0; s < 4; ++s) {
        const bf16x8 a16 = *(const bf16x8*)(sA + krow * 256 + (((4 * s + g) ^ (krow & 15)) << 4));
        const int k0 = 32 * s + 8 * g + q4;
        const bf16x8 b16 = cat8(tr_read(sC + k0 * 32 + 8 * p4), tr_read(sC + (k0 + 4) * 32 + 8 * p4));
        if (nt & 1) {
          c4 = __builtin_amdgcn_mfma_f32_16x16x32_bf16(a16, b16, c4, 0, 0, 0);
          cs4 = __builtin_amdgcn_mfma_f32_16x16x32_bf16(ones, b16, cs4, 0, 0, 0);
        } else {
          c4 = __builtin_amdgcn_mfma_f32_16x16x32_bf16(b16, a16, c4, 0, 0, 0);
          cs4 = __builtin_amdgcn_mfma_f32_16x16x32_bf16(b16, ones, cs4, 0, 0, 0);
        }
      }
    }
  }
  TS(0, 2);
#pragma unroll
  for (int i = 0; i < 2; ++i) {
    float s_ = ssum[i], q_ = ssq[i];
#pragma unroll
    for (int o = 1; o < 16; o <<= 1) { s_ += __shfl_xor(s_, o); q_ += __shfl_xor(q_, o); }
    if (ac == 0) {
      const float mean = s_ * (1.f / DM);
      const float var = fmaxf(q_ * (1.f / DM) - mean * mean, 0.f);
      sstat[(i * 32 + ar) * 2] = mean;
      sstat[(i * 32 + ar) * 2 + 1] = rsqrtf(var + 1e-5f);
    }
  }
  __syncthreads();
  const float mean = sstat[arow * 2], rstd = sstat[arow * 2 + 1];
#pragma unroll
  for (int i = 0; i < 16; ++i) acc[i] = fmaf(-mean, cs[i], acc[i]);
  float* red = (float*)smem + wq_ * 1024 + lane;
  if (kp == 1) {
#pragma unroll
    for (int i = 0; i < 16; ++i) red[i * 64] = acc[i];
  }
  if (w == 5) {
    float r[4];
    u32x2 o;
    if (nt & 1) {
#pragma unroll
      for (int i = 0; i < 4; ++i) {
        const int rr = rb + 4 * g + i;
        r[i] = (c4[i] - sstat[rr * 2] * cs4[i]) * sstat[rr * 2 + 1];
      }
      o[0] = cvtpk(r[0], r[1]); o[1] = cvtpk(r[2], r[3]);
      const int key0 = m0 + rb;
      const int t = key0 >> 5, s = (key0 >> 4) & 1;
      short* vf = (short*)(ws + WS_VF) + (size_t)t * 512 + s * 256 + (g & 1) * 128 + (lane & 15) * 8 + (g >> 1) * 4;
      *(u32x2*)vf = o;
    } else {
      const int rr = rb + (lane & 15);
      const float mn = sstat[rr * 2], rs = sstat[rr * 2 + 1];
#pragma unroll
      for (int i = 0; i < 4; ++i) r[i] = (c4[i] - mn * cs4[i]) * rs;
      o[0] = cvtpk(r[0], r[1]); o[1] = cvtpk(r[2], r[3]);
      const int key = m0 + rr;
      const int t = key >> 5;
      short* kf = (short*)(ws + WS_KF) + (size_t)t * 512 + (g >> 1) * 256 + (key & 31) * 8 + (g & 1) * 4;
      *(u32x2*)kf = o;
      const float k0 = bflo(o[0]), k1 = bfhi(o[0]), k2 = bflo(o[1]), k3 = bfhi(o[1]);
      float a = k0 * k0 + k1 * k1 + k2 * k2 + k3 * k3;
      a += __shfl_xor(a, 16);
      a += __shfl_xor(a, 32);
#pragma unroll
      for (int of = 8; of > 0; of >>= 1) a = fmaxf(a, __shfl_xor(a, of));
      if (lane == 0) ((float*)(ws + WS_KM))[mt * 4 + (nt >> 1)] = a;
    }
  }
  __syncthreads();
  if (kp == 1) return;
  const float sc = rstd * QSCALE;
  short* qo = (short*)(ws + WS_Q) + (size_t)(m0 + arow) * DM + n0 + wn * 32 + 4 * h;
#pragma unroll
  for (int gp = 0; gp < 2; ++gp) {
    u32x2 o[2];
#pragma unroll
    for (int e = 0; e < 2; ++e) {
      const int gg = 2 * gp + e;
      o[e][0] = cvtpk((acc[4 * gg] + red[(4 * gg) * 64]) * sc, (acc[4 * gg + 1] + red[(4 * gg + 1) * 64]) * sc);
      o[e][1] = cvtpk((acc[4 * gg + 2] + red[(4 * gg + 2) * 64]) * sc, (acc[4 * gg + 3] + red[(4 * gg + 3) * 64]) * sc);
    }
    *(u32x4*)(qo + 4 * h + 16 * gp) = widen16(o[0], o[1]);
  }
  TS(0, 3);
#undef K1_LOAD
#undef K1_WRITE
}

constexpr int TS = 2080;
constexpr int ATT_RED = 64 * TS;
constexpr int ATT_LDS = ATT_RED + 8 * 64 * 9 * 4 + 1024;

__global__ __launch_bounds__(1024) void k2_attn(const short* __restrict__ w1, const short* __restrict__ w3,
                                                const short* __restrict__ wo, const short* __restrict__ w2,
                                                char* __restrict__ ws) {
  extern __shared__ __attribute__((aligned(16))) char smem[];
  const int tid = threadIdx.x, lane = tid & 63, w = tid >> 6;
  const int r32 = lane & 31, h = lane >> 5;
  float* scomb = (float*)(smem + ATT_RED);
  TS(1, 0);
  const int wr = w & 7, kh = w >> 3;
  const int pblk = (blockIdx.x & 7) * 32 + (blockIdx.x >> 3);
  const size_t row = (size_t)pblk * 256 + wr * 32 + r32;
  const int tl0 = (w < 8) ? w : 24 + w;
  bf16x8 kv[8];
  {
    const bf16x8* srck = (const bf16x8*)(ws + WS_KF) + tl0 * 64 + lane;
    const bf16x8* srcv = (const bf16x8*)(ws + WS_VF) + tl0 * 64 + lane;
#pragma unroll
    for (int j = 0; j < 4; ++j) { kv[2 * j] = srck[j * 512]; kv[2 * j + 1] = srcv[j * 512]; }
  }
  const bf16x8 qf = *(const bf16x8*)((const short*)(ws + WS_Q) + row * 16 + h * 8);
  float kn2 = fmaxf(((const float*)(ws + WS_KM))[lane], ((const float*)(ws + WS_KM))[64 + lane]);
  bf16x8 csv0, csv1, rl;
  if (tid < 512) *(unsigned*)(smem + (tid >> 3) * TS + 2048 + (tid & 7) * 4) = (tid & 7) < 4 ? 0u : 0x3F803F80u;
  char* sdst = smem + tl0 * TS + lane * 16;
  *(bf16x8*)(sdst) = kv[0];
  *(bf16x8*)(sdst + 1024) = kv[1];
#pragma unroll
  for (int o = 32; o > 0; o >>= 1) kn2 = fmaxf(kn2, __shfl_xor(kn2, o));
  float qn2 = 0.f;
#pragma unroll
  for (int j = 0; j < 8; ++j) { const float f = bf2f(qf[j]); qn2 += f * f; }
  qn2 += __shfl_xor(qn2, 32);
  const float mref = sqrtf(qn2 * kn2) * 1.001f;

  const bool vlane = r32 < 16;
  const int kbase = lane * 16;
  const int vbase0 = vlane ? (1024 + h * 256 + r32 * 16) : (2048 + (r32 == 16 ? 16 : 0));
  const int vbase1 = vlane ? vbase0 + 512 : vbase0;
  f32x16 o = {};
  {
    f32x16 negm;
#pragma unroll
    for (int i = 0; i < 16; ++i) negm[i] = -mref;
    const char* kp = smem + kbase + kh * 32 * TS;
    const char* vp0 = smem + vbase0 + kh * 32 * TS;
    const char* vp1 = smem + vbase1 + kh * 32 * TS;
#pragma unroll
    for (int j = 0; j < 4; ++j) {
      __syncthreads();
      TS(1, 1 + j);
      if (j == 0) {
  {
    const int gi = blockIdx.x < 172 ? blockIdx.x : 0, t2 = tid & 511;
    const short* wsrc = ((gi < 86) ? (w1 + gi * 16) : (w3 + (gi - 86) * 16)) + (size_t)(t2 >> 1) * FF + (t2 & 1) * 8;
    csv0 = *(const bf16x8*)(wsrc);
    csv1 = *(const bf16x8*)(wsrc + (size_t)256 * FF);
  }
      }
      if (j < 3) {
        *(bf16x8*)(sdst + (j + 1) * 8 * TS) = kv[2 * j + 2];
        *(bf16x8*)(sdst + (j + 1) * 8 * TS + 1024) = kv[2 * j + 3];
      }
      if (j >= 1 && j <= 2) {
        const int u = blockIdx.x * 16 + w + (j - 1) * 4096;
        if (u < 4640) {
          char* scr = (char*)scomb + w * 1024;
          *(bf16x8*)(scr + (lane >> 2) * 64 + (lane & 3) * 16) = rl;
          const int g4 = lane >> 4;
          const char* rp = scr + (8 * (g4 >> 1) + ((lane & 15) >> 2)) * 64 + ((g4 & 1) * 16 + 4 * (lane & 3)) * 2;
          char* dst = ws + WS_WOF + (size_t)u * 1024;
          st16_wt(dst + lane * 16, __builtin_bit_cast(u32x4, cat8(tr_read(rp), tr_read(rp + 256))));
        }
      }
      if (j <= 1) {
        int u = blockIdx.x * 16 + w + j * 4096;
        u = u < 4640 ? u : 0;
        const int kk = lane >> 2, pc = lane & 3;
        const short* src;
        if (u < 512) {
          src = wo + (size_t)(16 * (u & 31) + kk) * DM + 32 * (u >> 5) + pc * 8;
        } else if (u < 3264) {
          const int v = u - 512;
          src = (pc < 2 ? w1 : w3) + (size_t)(16 * (v & 31) + kk) * FF + (v >> 5) * 16 + (pc & 1) * 8;
        } else {
          const int v = u - 3264;
          src = w2 + (size_t)(16 * (v % 86) + kk) * DM + 32 * (v / 86) + pc * 8;
        }
        rl = *(const bf16x8*)src;
      }
#pragma unroll
      for (int t = j * 8; t < j * 8 + 8; ++t) {
        const bf16x8 kf = *(const bf16x8*)(kp + t * TS);
        const bf16x8 vf0 = *(const bf16x8*)(vp0 + t * TS);
        const bf16x8 vf1 = *(const bf16x8*)(vp1 + t * TS);
        const f32x16 s = __builtin_amdgcn_mfma_f32_32x32x16_bf16(kf, qf, negm, 0, 0, 0);
        u32x4 p0, p1;
#pragma unroll
        for (int i = 0; i < 4; ++i) {
          p0[i] = cvtpk(__builtin_amdgcn_exp2f(s[2 * i]), __builtin_amdgcn_exp2f(s[2 * i + 1]));
          p1[i] = cvtpk(__builtin_amdgcn_exp2f(s[8 + 2 * i]), __builtin_amdgcn_exp2f(s[8 + 2 * i + 1]));
        }
        o = __builtin_amdgcn_mfma_f32_32x32x16_bf16(vf0, __builtin_bit_cast(bf16x8, p0), o, 0, 0, 0);
        o = __builtin_amdgcn_mfma_f32_32x32x16_bf16(vf1, __builtin_bit_cast(bf16x8, p1), o, 0, 0, 0);
      }
    }
  }
  TS(1, 5);
  TS(1, 6);
  float* cb = scomb + wr * (64 * 9) + lane * 9;
  if (kh == 1) {
#pragma unroll
    for (int i = 0; i < 9; ++i) cb[i] = o[i];
  }
  __syncthreads();
  if (kh == 1) {
    if (blockIdx.x < 172) {
      float cf[8];
#pragma unroll
      for (int j = 0; j < 8; ++j) {
        cf[j] = bf2f(csv0[j]) + bf2f(csv1[j]);
#pragma unroll
        for (int of = 2; of < 64; of <<= 1) cf[j] += __shfl_xor(cf[j], of);
      }
      if (lane < 2) {
        float* dst = (float*)(ws + WS_CS) + (size_t)(blockIdx.x * 8 + wr) * 16 + lane * 8;
        *(f32x4*)dst = f32x4{cf[0], cf[1], cf[2], cf[3]};
        *(f32x4*)(dst + 4) = f32x4{cf[4], cf[5], cf[6], cf[7]};
      }
    }
    return;
  }
#pragma unroll
  for (int i = 0; i < 9; ++i) o[i] += cb[i];
  float l = __shfl(o[8], r32);
  if (__any(l < 1e-18f)) {
    f32x16 negm = {};
    o = f32x16{};
#pragma unroll 1
    for (int t = 0; t < 64; ++t) {
      const bf16x8 kf = *(const bf16x8*)(smem + kbase + t * TS);
      const bf16x8 vf0 = *(const bf16x8*)(smem + vbase0 + t * TS);
      const bf16x8 vf1 = *(const bf16x8*)(smem + vbase1 + t * TS);
      f32x16 s = __builtin_amdgcn_mfma_f32_32x32x16_bf16(kf, qf, negm, 0, 0, 0);
      float mx = s[0];
#pragma unroll
      for (int i = 1; i < 16; ++i) mx = fmaxf(mx, s[i]);
      float d = fmaxf(mx, __shfl_xor(mx, 32));
      if (t != 0) d = fmaxf(d, 0.f);
      const float sc = (t != 0) ? __builtin_amdgcn_exp2f(-d) : 1.f;
#pragma unroll
      for (int i = 0; i < 16; ++i) { s[i] -= d; negm[i] -= d; o[i] *= sc; }
      u32x4 p0, p1;
#pragma unroll
      for (int i = 0; i < 4; ++i) {
        p0[i] = cvtpk(__builtin_amdgcn_exp2f(s[2 * i]), __builtin_amdgcn_exp2f(s[2 * i + 1]));
        p1[i] = cvtpk(__builtin_amdgcn_exp2f(s[8 + 2 * i]), __builtin_amdgcn_exp2f(s[8 + 2 * i + 1]));
      }
      o = __builtin_amdgcn_mfma_f32_32x32x16_bf16(vf0, __builtin_bit_cast(bf16x8, p0), o, 0, 0, 0);
      o = __builtin_amdgcn_mfma_f32_32x32x16_bf16(vf1, __builtin_bit_cast(bf16x8, p1), o, 0, 0, 0);
    }
    l = __shfl(o[8], r32);
  }
  const float inv = 1.f / l;
  short* op = (short*)(ws + WS_O) + row * 16 + 4 * h;
  u32x2 a, c;
  a[0] = cvtpk(o[0] * inv, o[1] * inv); a[1] = cvtpk(o[2] * inv, o[3] * inv);
  c[0] = cvtpk(o[4] * inv, o[5] * inv); c[1] = cvtpk(o[6] * inv, o[7] * inv);
  *(u32x4*)(op + 4 * h) = widen16(a, c);
  TS(1, 7);
}

constexpr int KG_LDS = 3 * 32768;
template <int KTOT, bool STATS>
__global__ __launch_bounds__(512) void k_gemm64(const short* __restrict__ A, const char* __restrict__ BF,
                                                const short* __restrict__ resid, short* __restrict__ out,
                                                char* __restrict__ ws) {
  extern __shared__ __attribute__((aligned(16))) char smem[];
  constexpr int NCH = (KTOT + 127) / 128;
  constexpr int KLAST = KTOT - (NCH - 1) * 128;
  constexpr int NKS = KTOT / 16;
  const int tid = threadIdx.x, lane = tid & 63, w = __builtin_amdgcn_readfirstlane(tid >> 6);
  const int b = blockIdx.x, xcd = b & 7, idx = b >> 3;
  const int mt = xcd * 4 + (idx & 3), nt = idx >> 2;
  const int m0 = mt * 64, n0 = nt * 64;
  TS((STATS ? 2 : 4), 0);
  const short* asrc[2];
  const char* bsrc[2];
  int aco[2];
#pragma unroll
  for (int i = 0; i < 2; ++i) {
    const int row = (w * 2 + i) * 4 + (lane >> 4);
    aco[i] = ((lane & 15) ^ (row & 15)) << 3;
    asrc[i] = A + (size_t)(m0 + row) * KTOT;
    const int e = w * 2 + i;
    bsrc[i] = BF + (size_t)((nt * 2 + (e >> 3)) * NKS + (e & 7)) * 1024 + lane * 16;
  }
  const unsigned lbase = (unsigned)(size_t)(lvoid_t*)smem;
#define KG_DMA(c) do { const unsigned st_ = lbase + ((c) % 3) * 32768; _Pragma("unroll") for (int i = 0; i < 2; ++i) { \
      const int ka = (((c) < NCH - 1) || (aco[i] < KLAST)) ? (c) * 128 + aco[i] : 0; \
      glds16(asrc[i] + ka, st_ + (w * 2 + i) * 1024); \
      const int kb = (((c) < NCH - 1) || (((w * 2 + i) & 7) * 16 < KLAST)) ? (c) * 8192 : 0; \
      glds16(bsrc[i] + kb, st_ + 16384 + (w * 2 + i) * 1024); } } while (0)
  KG_DMA(0); KG_DMA(1);

  const int r32 = lane & 31, h = lane >> 5;
  const int wq_ = w & 3, kp = w >> 2;
  const int wm = wq_ >> 1, wn = wq_ & 1;
  const int arow = wm * 32 + r32;
  const size_t off = (size_t)(m0 + arow) * DM + n0 + wn * 32 + 4 * h;
  s16x4 xr[4];
#pragma unroll
  for (int gg = 0; gg < 4; ++gg) xr[gg] = *(const s16x4*)(resid + off + 8 * gg);
  asm volatile("" :: "v"(xr[0]), "v"(xr[1]), "v"(xr[2]), "v"(xr[3]));
  f32x16 acc = {};
#pragma unroll
  for (int c = 0; c < NCH; ++c) {
    if (c < NCH - 1) asm volatile("s_waitcnt vmcnt(4)" ::: "memory");
    else             asm volatile("s_waitcnt vmcnt(0)" ::: "memory");
    asm volatile("s_waitcnt lgkmcnt(0)" ::: "memory");
    __builtin_amdgcn_s_barrier();
    if (c == 0) TS((STATS ? 2 : 4), 1);
    if (c + 2 < NCH) KG_DMA(c + 2);
    const char* sA = smem + (c % 3) * 32768;
    const char* sB = sA + 16384;
#pragma unroll
    for (int ss = 0; ss < 4; ++ss) {
      const int s = 2 * ss + kp;
      if (c < NCH - 1 || 16 * (2 * ss) < KLAST) {
        const bf16x8 af = *(const bf16x8*)(sA + arow * 256 + (((2 * s + h) ^ (arow & 15)) << 4));
        const bf16x8 bf = *(const bf16x8*)(sB + (wn * 8 + s) * 1024 + lane * 16);
        acc = __builtin_amdgcn_mfma_f32_32x32x16_bf16(bf, af, acc, 0, 0, 0);
      }
    }
  }
  TS((STATS ? 2 : 4), 2);
  float* red = (float*)(smem + (NCH % 3) * 32768) + wq_ * 1024 + lane;
  if (kp == 1) {
#pragma unroll
    for (int i = 0; i < 16; ++i) red[i * 64] = acc[i];
  }
  __syncthreads();
  if (kp == 1) return;
  float ssum = 0.f, ssq = 0.f;
#pragma unroll
  for (int gp = 0; gp < 2; ++gp) {
    u32x2 o[2];
#pragma unroll
    for (int e = 0; e < 2; ++e) {
      const int gg = 2 * gp + e;
      o[e][0] = cvtpk(acc[4 * gg] + red[(4 * gg) * 64] + bf2f(xr[gg][0]), acc[4 * gg + 1] + red[(4 * gg + 1) * 64] + bf2f(xr[gg][1]));
      o[e][1] = cvtpk(acc[4 * gg + 2] + red[(4 * gg + 2) * 64] + bf2f(xr[gg][2]), acc[4 * gg + 3] + red[(4 * gg + 3) * 64] + bf2f(xr[gg][3]));
      if (STATS) {
        stat2(o[e][0], ssum, ssq);
        stat2(o[e][1], ssum, ssq);
      }
    }
    *(u32x4*)(out + off + 4 * h + 16 * gp) = widen16(o[0], o[1]);
  }
  if (STATS) {
    ssum += __shfl_xor(ssum, 32);
    ssq += __shfl_xor(ssq, 32);
    if (h == 0) {
      f32x2 st = {ssum, ssq};
      *(f32x2*)((float*)(ws + WS_ST) + (size_t)(m0 + arow) * 32 + (nt * 2 + wn) * 2) = st;
    }
  }
  TS((STATS ? 2 : 4), 3);
#undef KG_DMA
}

constexpr int K4_ST = 40960;
constexpr int K4_LDS = 3 * K4_ST + 1024 + 1024;
__global__ __launch_bounds__(512) void k4_ffn1(char* __restrict__ ws) {
  extern __shared__ __attribute__((aligned(16))) char smem[];
  float* sstat = (float*)(smem + 3 * K4_ST);
  float* scs = sstat + 256;
  const int tid = threadIdx.x, lane = tid & 63, wall = __builtin_amdgcn_readfirstlane(tid >> 6);
  const int w = wall & 3;
  const bool loader = wall >= 4;
  const int b = blockIdx.x, xcd = b & 7, idx = b >> 3;
  const int mt = xcd * 2 + (idx & 1), ng = idx >> 1;
  const int m0 = mt * 128;
  const int cnt = ng < 6 ? 6 : 5;
  const int f0 = (ng * 5 + (ng < 6 ? ng : 6)) * 16;
  const short* H = (const short*)(ws + WS_H);
  TS(3, 0);
  const short* asrc[4];
  const short* bsrc[6];
#pragma unroll
  for (int i = 0; i < 4; ++i) {
    const int row = (w * 4 + i) * 8 + (lane >> 3);
    asrc[i] = H + (size_t)(m0 + row) * DM + (((lane & 7) ^ ((row >> 1) & 7)) << 3);
  }
#pragma unroll
  for (int i = 0; i < 6; ++i) {
    const int e = w * 6 + i, j = e >> 2, ks = e & 3;
    const int ct = (f0 >> 4) + (j < cnt ? j : 0);
    bsrc[i] = (const short*)(ws + WS_W13F + (size_t)(ct * 32 + ks) * 1024 + lane * 16);
  }
  const unsigned lbase = (unsigned)(size_t)(lvoid_t*)smem;
#define K4_DMA(c) do { const unsigned st_ = lbase + ((c) % 3) * K4_ST; \
    _Pragma("unroll") for (int i = 0; i < 4; ++i) glds16(asrc[i] + (c) * 64, st_ + (w * 4 + i) * 1024); \
    _Pragma("unroll") for (int i = 0; i < 6; ++i) glds16(bsrc[i] + (c) * 2048, st_ + 16384 + (w * 6 + i) * 1024); } while (0)
  if (loader) { K4_DMA(0); K4_DMA(1); }
  if (tid < 128) {
    const f32x4* sp = (const f32x4*)((const float*)(ws + WS_ST) + (size_t)(m0 + tid) * 32);
    float s_ = 0.f, q_ = 0.f;
#pragma unroll
    for (int i = 0; i < 8; ++i) { const f32x4 v = sp[i]; s_ += v[0] + v[2]; q_ += v[1] + v[3]; }
    const float mean = s_ * (1.f / DM);
    const float var = fmaxf(q_ * (1.f / DM) - mean * mean, 0.f);
    sstat[tid * 2] = mean;
    sstat[tid * 2 + 1] = rsqrtf(var + 1e-5f);
  }
  if (tid < 192) {
    const int e = tid, ct = e >> 5, isup = (e >> 4) & 1;
    const int gi = isup * 86 + (f0 >> 4) + (ct < cnt ? ct : 0);
    const float* cp = (const float*)(ws + WS_CS) + (size_t)gi * 128 + (e & 15);
    float a = 0.f;
#pragma unroll
    for (int i = 0; i < 8; ++i) a += cp[i * 16];
    scs[e] = a;
  }

  const int r32 = lane & 31, h = lane >> 5, g = lane >> 4, q4 = (lane & 15) >> 2, p4 = lane & 3;
  const int wm = w >> 1, wn = w & 1;
  if (loader) {
#pragma unroll
    for (int c = 0; c < 8; ++c) {
      if (c < 7) asm volatile("s_waitcnt vmcnt(10)" ::: "memory");
      else       asm volatile("s_waitcnt vmcnt(0)" ::: "memory");
      __builtin_amdgcn_s_barrier();
      if (c + 2 < 8) K4_DMA(c + 2);
    }
    return;
  }
  f32x16 acc[2][3] = {};
#pragma unroll
  for (int c = 0; c < 8; ++c) {
    asm volatile("s_waitcnt lgkmcnt(0)" ::: "memory");
    __builtin_amdgcn_s_barrier();
    if (c == 0) TS(3, 1);
    const char* sA = smem + (c % 3) * K4_ST;
    const char* sB = sA + 16384;
#pragma unroll
    for (int s = 0; s < 4; ++s) {
      bf16x8 af[2], bf[3];
#pragma unroll
      for (int i = 0; i < 2; ++i) {
        const int arow = wm * 64 + i * 32 + r32;
        af[i] = *(const bf16x8*)(sA + arow * 128 + (((2 * s + h) ^ ((arow >> 1) & 7)) << 4));
      }
#pragma unroll
      for (int j = 0; j < 3; ++j) bf[j] = *(const bf16x8*)(sB + ((wn * 3 + j) * 4 + s) * 1024 + lane * 16);
#pragma unroll
      for (int i = 0; i < 2; ++i)
#pragma unroll
        for (int j = 0; j < 3; ++j)
          acc[i][j] = __builtin_amdgcn_mfma_f32_32x32x16_bf16(bf[j], af[i], acc[i][j], 0, 0, 0);
    }
  }
  TS(3, 2);
#pragma unroll
  for (int i2 = 0; i2 < 2; ++i2) {
    const int arow = wm * 64 + i2 * 32 + r32;
    const float mean = sstat[arow * 2], rstd = sstat[arow * 2 + 1];
#pragma unroll
    for (int j = 0; j < 3; ++j) {
      if (wn * 3 + j < cnt) {
        const int fc = f0 + (wn * 3 + j) * 16 + 4 * h;
        const float* cp = scs + (wn * 3 + j) * 32 + 4 * h;
        const f32x4 g0 = *(const f32x4*)(cp), g1 = *(const f32x4*)(cp + 8);
        const f32x4 u0 = *(const f32x4*)(cp + 16), u1 = *(const f32x4*)(cp + 24);
        short* go = (short*)(ws + WS_G) + (size_t)(m0 + arow) * FF + fc;
        float r[8];
#pragma unroll
        for (int i = 0; i < 8; ++i) {
          const float gt = (acc[i2][j][i] - mean * (i < 4 ? g0[i & 3] : g1[i & 3])) * rstd;
          const float up = (acc[i2][j][i + 8] - mean * (i < 4 ? u0[i & 3] : u1[i & 3])) * rstd;
          r[i] = gt * __builtin_amdgcn_rcpf(1.f + __expf(-gt)) * up;
        }
        u32x2 a, c;
        a[0] = cvtpk(r[0], r[1]); a[1] = cvtpk(r[2], r[3]);
        c[0] = cvtpk(r[4], r[5]); c[1] = cvtpk(r[6], r[7]);
        *(u32x4*)(go + 4 * h) = widen16(a, c);
      }
    }
  }
  TS(3, 3);
#undef K4_DMA
}

extern "C" void kernel_launch(void* const* d_in, const int* in_sizes, int n_in, void* d_out, int out_size,
                              void* d_ws, size_t ws_size, hipStream_t stream) {
  const short* x = (const short*)d_in[0];
  const short* wq = (const short*)d_in[1];
  const short* wk = (const short*)d_in[2];
  const short* wv = (const short*)d_in[3];
  const short* wo = (const short*)d_in[4];
  const short* w1 = (const short*)d_in[6];
  const short* w3 = (const short*)d_in[7];
  const short* w2 = (const short*)d_in[8];
  char* ws = (char*)d_ws;
  (void)hipFuncSetAttribute((const void*)k1_ln_qkv, hipFuncAttributeMaxDynamicSharedMemorySize, K1_LDS);
  (void)hipFuncSetAttribute((const void*)k2_attn, hipFuncAttributeMaxDynamicSharedMemorySize, ATT_LDS);
  (void)hipFuncSetAttribute((const void*)k_gemm64<DM, true>, hipFuncAttributeMaxDynamicSharedMemorySize, KG_LDS);
  (void)hipFuncSetAttribute((const void*)k4_ffn1, hipFuncAttributeMaxDynamicSharedMemorySize, K4_LDS);
  (void)hipFuncSetAttribute((const void*)k_gemm64<FF, false>, hipFuncAttributeMaxDynamicSharedMemorySize, KG_LDS);
  k1_ln_qkv<<<256, 512, K1_LDS, stream>>>(x, wq, wk, wv, ws);
  k2_attn<<<256, 1024, ATT_LDS, stream>>>(w1, w3, wo, w2, ws);
  k_gemm64<DM, true><<<256, 512, KG_LDS, stream>>>((const short*)(ws + WS_O), ws + WS_WOF, x, (short*)(ws + WS_H), ws);
  k4_ffn1<<<256, 512, K4_LDS, stream>>>(ws);
  k_gemm64<FF, false><<<256, 512, KG_LDS, stream>>>((const short*)(ws + WS_G), ws + WS_W2F, (const short*)(ws + WS_H), (short*)d_out, ws);
}
```

```cpp
#include <hip/hip_runtime.h>
#include <hip/hip_bf16.h>

typedef __attribute__((ext_vector_type(8))) short bf16x8;
typedef __attribute__((ext_vector_type(4))) short s16x4;
typedef __attribute__((ext_vector_type(16))) float f32x16;
typedef __attribute__((ext_vector_type(4))) float f32x4;
typedef __attribute__((ext_vector_type(2))) float f32x2;
typedef __attribute__((ext_vector_type(4))) unsigned u32x4;
typedef __attribute__((ext_vector_type(2))) unsigned u32x2;
typedef __attribute__((ext_vector_type(2))) __bf16 bf16x2_t;
typedef __attribute__((address_space(3))) s16x4 lds_s16x4;

#define DI __device__ __forceinline__

constexpr int S_LEN = 2048, DM = 512, NH = 32, DH = 16, FF = 1376;
constexpr float QSCALE = 0.25f * 1.4426950408889634f;

constexpr size_t WS_Q = 0;
constexpr size_t WS_KF = WS_Q + (size_t)S_LEN * DM * 2;
constexpr size_t WS_VF = WS_KF + (size_t)S_LEN * DH * 2;
constexpr size_t WS_O = WS_VF + (size_t)S_LEN * DH * 2;
constexpr size_t WS_H = WS_O + (size_t)S_LEN * DM * 2;
constexpr size_t WS_G = WS_H + (size_t)S_LEN * DM * 2;
constexpr size_t WS_KM = WS_G + (size_t)S_LEN * FF * 2;
constexpr size_t WS_ST = WS_KM + 512;
constexpr size_t WS_CS = WS_ST + (size_t)S_LEN * 32 * 4;
constexpr size_t WS_WOF = WS_CS + 172 * 128 * 4;
constexpr size_t WS_W13F = WS_WOF + 512 * 1024;
constexpr size_t WS_W2F = WS_W13F + 2752 * 1024;

DI unsigned cvtpk(float lo, float hi) {
  const f32x2 v = {lo, hi};
  return __builtin_bit_cast(unsigned, __builtin_convertvector(v, bf16x2_t));
}
DI float bf2f(short v) { return __uint_as_float(((unsigned)(unsigned short)v) << 16); }
DI float bflo(unsigned u) { return __uint_as_float(u << 16); }
DI float bfhi(unsigned u) { return __uint_as_float(u & 0xffff0000u); }
DI s16x4 tr_read(const char* p) {
  return __builtin_amdgcn_ds_read_tr16_b64_v4i16((lds_s16x4*)p);
}
DI bf16x8 cat8(s16x4 a, s16x4 b) { return __builtin_shufflevector(a, b, 0, 1, 2, 3, 4, 5, 6, 7); }
DI void stat2(unsigned u, float& s, float& q) {
  const float a = bflo(u), b = bfhi(u);
  s += a + b;
  q = fmaf(a, a, fmaf(b, b, q));
}
DI void stat8(const bf16x8& v, float& s, float& q) {
  const u32x4 u = __builtin_bit_cast(u32x4, v);
#pragma unroll
  for (int j = 0; j < 4; ++j) stat2(u[j], s, q);
}
typedef __attribute__((address_space(3))) void lvoid_t;
DI void glds16(const void* gsrc, unsigned lds_dst) {
  unsigned keep;
  asm volatile("s_mov_b32 %0, m0\n\ts_mov_b32 m0, %2\n\ts_nop 0\n\tglobal_load_lds_dwordx4 %1, off\n\ts_mov_b32 m0, %0"
               : "=&s"(keep) : "v"(gsrc), "s"(lds_dst) : "memory");
}
DI void st16_wt(void* p, u32x4 v) {
  asm volatile("global_store_dwordx4 %0, %1, off sc1\n\ts_nop 1" :: "v"(p), "v"(v) : "memory");
}
DI u32x4 widen16(u32x2 a, u32x2 b) {
  const auto r0 = __builtin_amdgcn_permlane32_swap(a[0], b[0], false, false);
  const auto r1 = __builtin_amdgcn_permlane32_swap(a[1], b[1], false, false);
  return u32x4{r0[0], r1[0], r0[1], r1[1]};
}
#define TS(kid, i)

constexpr int PD = 4;

constexpr int K1_ST = 36864;
constexpr int K1_LDS = 2 * K1_ST + 1024;
__global__ __launch_bounds__(512) void k1_ln_qkv(const short* __restrict__ x, const short* __restrict__ wq,
                                                 const short* __restrict__ wk, const short* __restrict__ wv,
                                                 char* __restrict__ ws) {
  extern __shared__ __attribute__((aligned(16))) char smem[];
  float* sstat = (float*)(smem + 2 * K1_ST);
  const int tid = threadIdx.x, lane = tid & 63, w = tid >> 6;
  const int b = blockIdx.x, xcd = b & 7, idx = b >> 3;
  const int mt = xcd * 4 + (idx & 3), nt = idx >> 2;
  const int m0 = mt * 64, n0 = nt * 64;
  const int ar = tid >> 4, ac = tid & 15, bk = tid >> 3, bc = tid & 7;
  const short* wkv = (nt & 1) ? wv : wk;
  TS(0, 0);
  bf16x8 av[PD][2], bv[PD][2], cv[PD];
#define K1_LOAD(c) do { _Pragma("unroll") for (int i = 0; i < 2; ++i) { \
      av[(c) % PD][i] = *(const bf16x8*)(x + (size_t)(m0 + i * 32 + ar) * DM + (c) * 128 + ac * 8); \
      bv[(c) % PD][i] = *(const bf16x8*)(wq + (size_t)((c) * 128 + i * 64 + bk) * DM + n0 + bc * 8); } \
    cv[(c) % PD] = *(const bf16x8*)(wkv + (size_t)(c) * 2048 + (tid & 255) * 8); } while (0)
#define K1_WRITE(c) do { char* sA_ = smem + ((c) & 1) * K1_ST; _Pragma("unroll") for (int i = 0; i < 2; ++i) { \
      const int row = i * 32 + ar; \
      *(bf16x8*)(sA_ + row * 256 + ((ac ^ (row & 15)) << 4)) = av[(c) % PD][i]; \
      stat8(av[(c) % PD][i], ssum[i], ssq[i]); \
      const int k = i * 64 + bk; \
      *(bf16x8*)(sA_ + 16384 + k * 128 + ((bc << 4) ^ (((k >> 1) & 1) << 6))) = bv[(c) % PD][i]; } \
    *(bf16x8*)(sA_ + 32768 + (tid & 255) * 16) = cv[(c) % PD]; } while (0)
  float ssum[2] = {0.f, 0.f}, ssq[2] = {0.f, 0.f};
  K1_LOAD(0); K1_LOAD(1); K1_LOAD(2); K1_LOAD(3);
  asm volatile("" ::: "memory");
  K1_WRITE(0);

  const int r32 = lane & 31, h = lane >> 5, g = lane >> 4, q4 = (lane & 15) >> 2, p4 = lane & 3;
  const int wq_ = w & 3, kp = w >> 2;
  const int wm = wq_ >> 1, wn = wq_ & 1;
  const int arow = wm * 32 + r32;
  const int nb = (wn * 32 + (g & 1) * 16 + 4 * p4) * 2;
  const int rb = (nt >> 1) * 16;
  bf16x8 ones;
#pragma unroll
  for (int j = 0; j < 8; ++j) ones[j] = (short)0x3F80;
  f32x16 acc = {}, cs = {};
  f32x4 c4 = {}, cs4 = {};
#pragma unroll
  for (int c = 0; c < 4; ++c) {
    __syncthreads();
    if (c == 0) TS(0, 1);
    const char* sA = smem + (c & 1) * K1_ST;
    const char* sB = sA + 16384;
    bf16x8 af[4], bf[4];
#pragma unroll
    for (int ss = 0; ss < 4; ++ss) {
      const int s = 2 * ss + kp;
      af[ss] = *(const bf16x8*)(sA + arow * 256 + (((2 * s + h) ^ (arow & 15)) << 4));
      const int k0 = 16 * s + 8 * h + q4;
      bf[ss] = cat8(tr_read(sB + k0 * 128 + (nb ^ (((k0 >> 1) & 1) << 6))),
                    tr_read(sB + (k0 + 4) * 128 + (nb ^ ((((k0 + 4) >> 1) & 1) << 6))));
    }
    if (c < 3) { K1_WRITE(c + 1); }
#pragma unroll
    for (int ss = 0; ss < 4; ++ss) {
      acc = __builtin_amdgcn_mfma_f32_32x32x16_bf16(bf[ss], af[ss], acc, 0, 0, 0);
      cs = __builtin_amdgcn_mfma_f32_32x32x16_bf16(bf[ss], ones, cs, 0, 0, 0);
    }
    if (w == 5) {
      const char* sC = sA + 32768;
      const int krow = rb + (lane & 15);
#pragma unroll
      for (int s = 0; s < 4; ++s) {
        const bf16x8 a16 = *(const bf16x8*)(sA + krow * 256 + (((4 * s + g) ^ (krow & 15)) << 4));
        const int k0 = 32 * s + 8 * g + q4;
        const bf16x8 b16 = cat8(tr_read(sC + k0 * 32 + 8 * p4), tr_read(sC + (k0 + 4) * 32 + 8 * p4));
        if (nt & 1) {
          c4 = __builtin_amdgcn_mfma_f32_16x16x32_bf16(a16, b16, c4, 0, 0, 0);
          cs4 = __builtin_amdgcn_mfma_f32_16x16x32_bf16(ones, b16, cs4, 0, 0, 0);
        } else {
          c4 = __builtin_amdgcn_mfma_f32_16x16x32_bf16(b16, a16, c4, 0, 0, 0);
          cs4 = __builtin_amdgcn_mfma_f32_16x16x32_bf16(b16, ones, cs4, 0, 0, 0);
        }
      }
    }
  }
  TS(0, 2);
#pragma unroll
  for (int i = 0; i < 2; ++i) {
    float s_ = ssum[i], q_ = ssq[i];
#pragma unroll
    for (int o = 1; o < 16; o <<= 1) { s_ += __shfl_xor(s_, o); q_ += __shfl_xor(q_, o); }
    if (ac == 0) {
      const float mean = s_ * (1.f / DM);
      const float var = fmaxf(q_ * (1.f / DM) - mean * mean, 0.f);
      sstat[(i * 32 + ar) * 2] = mean;
      sstat[(i * 32 + ar) * 2 + 1] = rsqrtf(var + 1e-5f);
    }
  }
  __syncthreads();
  const float mean = sstat[arow * 2], rstd = sstat[arow * 2 + 1];
#pragma unroll
  for (int i = 0; i < 16; ++i) acc[i] = fmaf(-mean, cs[i], acc[i]);
  float* red = (float*)smem + wq_ * 1024 + lane;
  if (kp == 1) {
#pragma unroll
    for (int i = 0; i < 16; ++i) red[i * 64] = acc[i];
  }
  if (w == 5) {
    float r[4];
    u32x2 o;
    if (nt & 1) {
#pragma unroll
      for (int i = 0; i < 4; ++i) {
        const int rr = rb + 4 * g + i;
        r[i] = (c4[i] - sstat[rr * 2] * cs4[i]) * sstat[rr * 2 + 1];
      }
      o[0] = cvtpk(r[0], r[1]); o[1] = cvtpk(r[2], r[3]);
      const int key0 = m0 + rb;
      const int t = key0 >> 5, s = (key0 >> 4) & 1;
      short* vf = (short*)(ws + WS_VF) + (size_t)t * 512 + s * 256 + (g & 1) * 128 + (lane & 15) * 8 + (g >> 1) * 4;
      *(u32x2*)vf = o;
    } else {
      const int rr = rb + (lane & 15);
      const float mn = sstat[rr * 2], rs = sstat[rr * 2 + 1];
#pragma unroll
      for (int i = 0; i < 4; ++i) r[i] = (c4[i] - mn * cs4[i]) * rs;
      o[0] = cvtpk(r[0], r[1]); o[1] = cvtpk(r[2], r[3]);
      const int key = m0 + rr;
      const int t = key >> 5;
      short* kf = (short*)(ws + WS_KF) + (size_t)t * 512 + (g >> 1) * 256 + (key & 31) * 8 + (g & 1) * 4;
      *(u32x2*)kf = o;
      const float k0 = bflo(o[0]), k1 = bfhi(o[0]), k2 = bflo(o[1]), k3 = bfhi(o[1]);
      float a = k0 * k0 + k1 * k1 + k2 * k2 + k3 * k3;
      a += __shfl_xor(a, 16);
      a += __shfl_xor(a, 32);
#pragma unroll
      for (int of = 8; of > 0; of >>= 1) a = fmaxf(a, __shfl_xor(a, of));
      if (lane == 0) ((float*)(ws + WS_KM))[mt * 4 + (nt >> 1)] = a;
    }
  }
  __syncthreads();
  if (kp == 1) return;
  const float sc = rstd * QSCALE;
  short* qo = (short*)(ws + WS_Q) + (size_t)(m0 + arow) * DM + n0 + wn * 32 + 4 * h;
#pragma unroll
  for (int gp = 0; gp < 2; ++gp) {
    u32x2 o[2];
#pragma unroll
    for (int e = 0; e < 2; ++e) {
      const int gg = 2 * gp + e;
      o[e][0] = cvtpk((acc[4 * gg] + red[(4 * gg) * 64]) * sc, (acc[4 * gg + 1] + red[(4 * gg + 1) * 64]) * sc);
      o[e][1] = cvtpk((acc[4 * gg + 2] + red[(4 * gg + 2) * 64]) * sc, (acc[4 * gg + 3] + red[(4 * gg + 3) * 64]) * sc);
    }
    *(u32x4*)(qo + 4 * h + 16 * gp) = widen16(o[0], o[1]);
  }
  TS(0, 3);
#undef K1_LOAD
#undef K1_WRITE
}

constexpr int TS = 2080;
constexpr int ATT_RED = 64 * TS;
constexpr int ATT_LDS = ATT_RED + 8 * 64 * 9 * 4 + 1024;

__global__ __launch_bounds__(1024) void k2_attn(const short* __restrict__ w1, const short* __restrict__ w3,
                                                const short* __restrict__ wo, const short* __restrict__ w2,
                                                char* __restrict__ ws) {
  extern __shared__ __attribute__((aligned(16))) char smem[];
  const int tid = threadIdx.x, lane = tid & 63, w = tid >> 6;
  const int r32 = lane & 31, h = lane >> 5;
  float* scomb = (float*)(smem + ATT_RED);
  TS(1, 0);
  const int wr = w & 7, kh = w >> 3;
  const int pblk = (blockIdx.x & 7) * 32 + (blockIdx.x >> 3);
  const size_t row = (size_t)pblk * 256 + wr * 32 + r32;
  const int tl0 = (w < 8) ? w : 24 + w;
  bf16x8 kv[8];
  {
    const bf16x8* srck = (const bf16x8*)(ws + WS_KF) + tl0 * 64 + lane;
    const bf16x8* srcv = (const bf16x8*)(ws + WS_VF) + tl0 * 64 + lane;
#pragma unroll
    for (int j = 0; j < 4; ++j) { kv[2 * j] = srck[j * 512]; kv[2 * j + 1] = srcv[j * 512]; }
  }
  const bf16x8 qf = *(const bf16x8*)((const short*)(ws + WS_Q) + row * 16 + h * 8);
  float kn2 = fmaxf(((const float*)(ws + WS_KM))[lane], ((const float*)(ws + WS_KM))[64 + lane]);
  bf16x8 csv0, csv1, rl;
  if (tid < 512) *(unsigned*)(smem + (tid >> 3) * TS + 2048 + (tid & 7) * 4) = (tid & 7) < 4 ? 0u : 0x3F803F80u;
  char* sdst = smem + tl0 * TS + lane * 16;
#pragma unroll
  for (int j = 0; j < 4; ++j) {
    *(bf16x8*)(sdst + j * 8 * TS) = kv[2 * j];
    *(bf16x8*)(sdst + j * 8 * TS + 1024) = kv[2 * j + 1];
  }
#pragma unroll
  for (int o = 32; o > 0; o >>= 1) kn2 = fmaxf(kn2, __shfl_xor(kn2, o));
  float qn2 = 0.f;
#pragma unroll
  for (int j = 0; j < 8; ++j) { const float f = bf2f(qf[j]); qn2 += f * f; }
  qn2 += __shfl_xor(qn2, 32);
  const float mref = sqrtf(qn2 * kn2) * 1.001f;

  const bool vlane = r32 < 16;
  const int kbase = lane * 16;
  const int vbase0 = vlane ? (1024 + h * 256 + r32 * 16) : (2048 + (r32 == 16 ? 16 : 0));
  const int vbase1 = vlane ? vbase0 + 512 : vbase0;
  f32x16 o = {};
  {
    f32x16 negm;
#pragma unroll
    for (int i = 0; i < 16; ++i) negm[i] = -mref;
    const char* kp = smem + kbase + kh * 32 * TS;
    const char* vp0 = smem + vbase0 + kh * 32 * TS;
    const char* vp1 = smem + vbase1 + kh * 32 * TS;
#pragma unroll
    for (int j = 0; j < 4; ++j) {
      if (j == 0) __syncthreads();
      TS(1, 1 + j);
      if (j == 0) {
  {
    const int gi = blockIdx.x < 172 ? blockIdx.x : 0, t2 = tid & 511;
    const short* wsrc = ((gi < 86) ? (w1 + gi * 16) : (w3 + (gi - 86) * 16)) + (size_t)(t2 >> 1) * FF + (t2 & 1) * 8;
    csv0 = *(const bf16x8*)(wsrc);
    csv1 = *(const bf16x8*)(wsrc + (size_t)256 * FF);
  }
      }
      if (j >= 1 && j <= 2) {
        const int u = blockIdx.x * 16 + w + (j - 1) * 4096;
        if (u < 4640) {
          char* scr = (char*)scomb + w * 1024;
          *(bf16x8*)(scr + (lane >> 2) * 64 + (lane & 3) * 16) = rl;
          const int g4 = lane >> 4;
          const char* rp = scr + (8 * (g4 >> 1) + ((lane & 15) >> 2)) * 64 + ((g4 & 1) * 16 + 4 * (lane & 3)) * 2;
          char* dst = ws + WS_WOF + (size_t)u * 1024;
          st16_wt(dst + lane * 16, __builtin_bit_cast(u32x4, cat8(tr_read(rp), tr_read(rp + 256))));
        }
      }
      if (j <= 1) {
        int u = blockIdx.x * 16 + w + j * 4096;
        u = u < 4640 ? u : 0;
        const int kk = lane >> 2, pc = lane & 3;
        const short* src;
        if (u < 512) {
          src = wo + (size_t)(16 * (u & 31) + kk) * DM + 32 * (u >> 5) + pc * 8;
        } else if (u < 3264) {
          const int v = u - 512;
          src = (pc < 2 ? w1 : w3) + (size_t)(16 * (v & 31) + kk) * FF + (v >> 5) * 16 + (pc & 1) * 8;
        } else {
          const int v = u - 3264;
          src = w2 + (size_t)(16 * (v % 86) + kk) * DM + 32 * (v / 86) + pc * 8;
        }
        rl = *(const bf16x8*)src;
      }
#pragma unroll
      for (int t = j * 8; t < j * 8 + 8; ++t) {
        const bf16x8 kf = *(const bf16x8*)(kp + t * TS);
        const bf16x8 vf0 = *(const bf16x8*)(vp0 + t * TS);
        const bf16x8 vf1 = *(const bf16x8*)(vp1 + t * TS);
        const f32x16 s = __builtin_amdgcn_mfma_f32_32x32x16_bf16(kf, qf, negm, 0, 0, 0);
        u32x4 p0, p1;
#pragma unroll
        for (int i = 0; i < 4; ++i) {
          p0[i] = cvtpk(__builtin_amdgcn_exp2f(s[2 * i]), __builtin_amdgcn_exp2f(s[2 * i + 1]));
          p1[i] = cvtpk(__builtin_amdgcn_exp2f(s[8 + 2 * i]), __builtin_amdgcn_exp2f(s[8 + 2 * i + 1]));
        }
        o = __builtin_amdgcn_mfma_f32_32x32x16_bf16(vf0, __builtin_bit_cast(bf16x8, p0), o, 0, 0, 0);
        o = __builtin_amdgcn_mfma_f32_32x32x16_bf16(vf1, __builtin_bit_cast(bf16x8, p1), o, 0, 0, 0);
      }
    }
  }
  TS(1, 5);
  TS(1, 6);
  float* cb = scomb + wr * (64 * 9) + lane * 9;
  if (kh == 1) {
#pragma unroll
    for (int i = 0; i < 9; ++i) cb[i] = o[i];
  }
  __syncthreads();
  if (kh == 1) {
    if (blockIdx.x < 172) {
      float cf[8];
#pragma unroll
      for (int j = 0; j < 8; ++j) {
        cf[j] = bf2f(csv0[j]) + bf2f(csv1[j]);
#pragma unroll
        for (int of = 2; of < 64; of <<= 1) cf[j] += __shfl_xor(cf[j], of);
      }
      if (lane < 2) {
        float* dst = (float*)(ws + WS_CS) + (size_t)(blockIdx.x * 8 + wr) * 16 + lane * 8;
        *(f32x4*)dst = f32x4{cf[0], cf[1], cf[2], cf[3]};
        *(f32x4*)(dst + 4) = f32x4{cf[4], cf[5], cf[6], cf[7]};
      }
    }
    return;
  }
#pragma unroll
  for (int i = 0; i < 9; ++i) o[i] += cb[i];
  float l = __shfl(o[8], r32);
  if (__any(l < 1e-18f)) {
    f32x16 negm = {};
    o = f32x16{};
#pragma unroll 1
    for (int t = 0; t < 64; ++t) {
      const bf16x8 kf = *(const bf16x8*)(smem + kbase + t * TS);
      const bf16x8 vf0 = *(const bf16x8*)(smem + vbase0 + t * TS);
      const bf16x8 vf1 = *(const bf16x8*)(smem + vbase1 + t * TS);
      f32x16 s = __builtin_amdgcn_mfma_f32_32x32x16_bf16(kf, qf, negm, 0, 0, 0);
      float mx = s[0];
#pragma unroll
      for (int i = 1; i < 16; ++i) mx = fmaxf(mx, s[i]);
      float d = fmaxf(mx, __shfl_xor(mx, 32));
      if (t != 0) d = fmaxf(d, 0.f);
      const float sc = (t != 0) ? __builtin_amdgcn_exp2f(-d) : 1.f;
#pragma unroll
      for (int i = 0; i < 16; ++i) { s[i] -= d; negm[i] -= d; o[i] *= sc; }
      u32x4 p0, p1;
#pragma unroll
      for (int i = 0; i < 4; ++i) {
        p0[i] = cvtpk(__builtin_amdgcn_exp2f(s[2 * i]), __builtin_amdgcn_exp2f(s[2 * i + 1]));
        p1[i] = cvtpk(__builtin_amdgcn_exp2f(s[8 + 2 * i]), __builtin_amdgcn_exp2f(s[8 + 2 * i + 1]));
      }
      o = __builtin_amdgcn_mfma_f32_32x32x16_bf16(vf0, __builtin_bit_cast(bf16x8, p0), o, 0, 0, 0);
      o = __builtin_amdgcn_mfma_f32_32x32x16_bf16(vf1, __builtin_bit_cast(bf16x8, p1), o, 0, 0, 0);
    }
    l = __shfl(o[8], r32);
  }
  const float inv = 1.f / l;
  short* op = (short*)(ws + WS_O) + row * 16 + 4 * h;
  u32x2 a, c;
  a[0] = cvtpk(o[0] * inv, o[1] * inv); a[1] = cvtpk(o[2] * inv, o[3] * inv);
  c[0] = cvtpk(o[4] * inv, o[5] * inv); c[1] = cvtpk(o[6] * inv, o[7] * inv);
  *(u32x4*)(op + 4 * h) = widen16(a, c);
  TS(1, 7);
}

constexpr int KG_LDS = 4 * 32768;
template <int KTOT, bool STATS>
__global__ __launch_bounds__(512) void k_gemm64(const short* __restrict__ A, const char* __restrict__ BF,
                                                const short* __restrict__ resid, short* __restrict__ out,
                                                char* __restrict__ ws) {
  extern __shared__ __attribute__((aligned(16))) char smem[];
  constexpr int NCH = (KTOT + 127) / 128;
  constexpr int KLAST = KTOT - (NCH - 1) * 128;
  constexpr int NKS = KTOT / 16;
  constexpr int NST = 4, DD = 3;
  const int tid = threadIdx.x, lane = tid & 63, w = __builtin_amdgcn_readfirstlane(tid >> 6);
  const int b = blockIdx.x, xcd = b & 7, idx = b >> 3;
  const int mt = xcd * 4 + (idx & 3), nt = idx >> 2;
  const int m0 = mt * 64, n0 = nt * 64;
  TS((STATS ? 2 : 4), 0);
  const short* asrc[2];
  const char* bsrc[2];
  int aco[2];
#pragma unroll
  for (int i = 0; i < 2; ++i) {
    const int row = (w * 2 + i) * 4 + (lane >> 4);
    aco[i] = ((lane & 15) ^ (row & 15)) << 3;
    asrc[i] = A + (size_t)(m0 + row) * KTOT;
    const int e = w * 2 + i;
    bsrc[i] = BF + (size_t)((nt * 2 + (e >> 3)) * NKS + (e & 7)) * 1024 + lane * 16;
  }
  const unsigned lbase = (unsigned)(size_t)(lvoid_t*)smem;
#define KG_DMA(c) do { const unsigned st_ = lbase + ((c) % NST) * 32768; _Pragma("unroll") for (int i = 0; i < 2; ++i) { \
      const int ka = (((c) < NCH - 1) || (aco[i] < KLAST)) ? (c) * 128 + aco[i] : 0; \
      glds16(asrc[i] + ka, st_ + (w * 2 + i) * 1024); \
      const int kb = (((c) < NCH - 1) || (((w * 2 + i) & 7) * 16 < KLAST)) ? (c) * 8192 : 0; \
      glds16(bsrc[i] + kb, st_ + 16384 + (w * 2 + i) * 1024); } } while (0)
  KG_DMA(0); KG_DMA(1); KG_DMA(2);

  const int r32 = lane & 31, h = lane >> 5;
  const int wq_ = w & 3, kp = w >> 2;
  const int wm = wq_ >> 1, wn = wq_ & 1;
  const int arow = wm * 32 + r32;
  const size_t off = (size_t)(m0 + arow) * DM + n0 + wn * 32 + 4 * h;
  s16x4 xr[4];
#pragma unroll
  for (int gg = 0; gg < 4; ++gg) xr[gg] = *(const s16x4*)(resid + off + 8 * gg);
  asm volatile("" :: "v"(xr[0]), "v"(xr[1]), "v"(xr[2]), "v"(xr[3]));
  f32x16 acc = {};
#pragma unroll
  for (int c = 0; c < NCH; ++c) {
    if (c < NCH - 2)       asm volatile("s_waitcnt vmcnt(8)" ::: "memory");
    else if (c == NCH - 2) asm volatile("s_waitcnt vmcnt(4)" ::: "memory");
    else                   asm volatile("s_waitcnt vmcnt(0)" ::: "memory");
    asm volatile("s_waitcnt lgkmcnt(0)" ::: "memory");
    __builtin_amdgcn_s_barrier();
    if (c == 0) TS((STATS ? 2 : 4), 1);
    if (c + DD < NCH) KG_DMA(c + DD);
    const char* sA = smem + (c % NST) * 32768;
    const char* sB = sA + 16384;
#pragma unroll
    for (int ss = 0; ss < 4; ++ss) {
      const int s = 2 * ss + kp;
      if (c < NCH - 1 || 16 * (2 * ss) < KLAST) {
        const bf16x8 af = *(const bf16x8*)(sA + arow * 256 + (((2 * s + h) ^ (arow & 15)) << 4));
        const bf16x8 bf = *(const bf16x8*)(sB + (wn * 8 + s) * 1024 + lane * 16);
        acc = __builtin_amdgcn_mfma_f32_32x32x16_bf16(bf, af, acc, 0, 0, 0);
      }
    }
  }
  TS((STATS ? 2 : 4), 2);
  float* red = (float*)(smem + (NCH % NST) * 32768) + wq_ * 1024 + lane;
  if (kp == 1) {
#pragma unroll
    for (int i = 0; i < 16; ++i) red[i * 64] = acc[i];
  }
  __syncthreads();
  if (kp == 1) return;
  float ssum = 0.f, ssq = 0.f;
#pragma unroll
  for (int gp = 0; gp < 2; ++gp) {
    u32x2 o[2];
#pragma unroll
    for (int e = 0; e < 2; ++e) {
      const int gg = 2 * gp + e;
      o[e][0] = cvtpk(acc[4 * gg] + red[(4 * gg) * 64] + bf2f(xr[gg][0]), acc[4 * gg + 1] + red[(4 * gg + 1) * 64] + bf2f(xr[gg][1]));
      o[e][1] = cvtpk(acc[4 * gg + 2] + red[(4 * gg + 2) * 64] + bf2f(xr[gg][2]), acc[4 * gg + 3] + red[(4 * gg + 3) * 64] + bf2f(xr[gg][3]));
      if (STATS) {
        stat2(o[e][0], ssum, ssq);
        stat2(o[e][1], ssum, ssq);
      }
    }
    *(u32x4*)(out + off + 4 * h + 16 * gp) = widen16(o[0], o[1]);
  }
  if (STATS) {
    ssum += __shfl_xor(ssum, 32);
    ssq += __shfl_xor(ssq, 32);
    if (h == 0) {
      f32x2 st = {ssum, ssq};
      *(f32x2*)((float*)(ws + WS_ST) + (size_t)(m0 + arow) * 32 + (nt * 2 + wn) * 2) = st;
    }
  }
  TS((STATS ? 2 : 4), 3);
#undef KG_DMA
}

constexpr int K4_ST = 40960;
constexpr int K4_LDS = 3 * K4_ST + 1024 + 1024;
__global__ __launch_bounds__(512) void k4_ffn1(char* __restrict__ ws) {
  extern __shared__ __attribute__((aligned(16))) char smem[];
  float* sstat = (float*)(smem + 3 * K4_ST);
  float* scs = sstat + 256;
  const int tid = threadIdx.x, lane = tid & 63, wall = __builtin_amdgcn_readfirstlane(tid >> 6);
  const int w = wall & 3;
  const bool loader = wall >= 4;
  const int b = blockIdx.x, xcd = b & 7, idx = b >> 3;
  const int mt = xcd * 2 + (idx & 1), ng = idx >> 1;
  const int m0 = mt * 128;
  const int cnt = ng < 6 ? 6 : 5;
  const int f0 = (ng * 5 + (ng < 6 ? ng : 6)) * 16;
  const short* H = (const short*)(ws + WS_H);
  TS(3, 0);
  const short* asrc[4];
  const short* bsrc[6];
#pragma unroll
  for (int i = 0; i < 4; ++i) {
    const int row = (w * 4 + i) * 8 + (lane >> 3);
    asrc[i] = H + (size_t)(m0 + row) * DM + (((lane & 7) ^ ((row >> 1) & 7)) << 3);
  }
#pragma unroll
  for (int i = 0; i < 6; ++i) {
    const int e = w * 6 + i, j = e >> 2, ks = e & 3;
    const int ct = (f0 >> 4) + (j < cnt ? j : 0);
    bsrc[i] = (const short*)(ws + WS_W13F + (size_t)(ct * 32 + ks) * 1024 + lane * 16);
  }
  const unsigned lbase = (unsigned)(size_t)(lvoid_t*)smem;
#define K4_DMA(c) do { const unsigned st_ = lbase + ((c) % 3) * K4_ST; \
    _Pragma("unroll") for (int i = 0; i < 4; ++i) glds16(asrc[i] + (c) * 64, st_ + (w * 4 + i) * 1024); \
    _Pragma("unroll") for (int i = 0; i < 6; ++i) glds16(bsrc[i] + (c) * 2048, st_ + 16384 + (w * 6 + i) * 1024); } while (0)
  if (loader) { K4_DMA(0); K4_DMA(1); }
  if (tid < 128) {
    const f32x4* sp = (const f32x4*)((const float*)(ws + WS_ST) + (size_t)(m0 + tid) * 32);
    float s_ = 0.f, q_ = 0.f;
#pragma unroll
    for (int i = 0; i < 8; ++i) { const f32x4 v = sp[i]; s_ += v[0] + v[2]; q_ += v[1] + v[3]; }
    const float mean = s_ * (1.f / DM);
    const float var = fmaxf(q_ * (1.f / DM) - mean * mean, 0.f);
    sstat[tid * 2] = mean;
    sstat[tid * 2 + 1] = rsqrtf(var + 1e-5f);
  }
  if (tid < 192) {
    const int e = tid, ct = e >> 5, isup = (e >> 4) & 1;
    const int gi = isup * 86 + (f0 >> 4) + (ct < cnt ? ct : 0);
    const float* cp = (const float*)(ws + WS_CS) + (size_t)gi * 128 + (e & 15);
    float a = 0.f;
#pragma unroll
    for (int i = 0; i < 8; ++i) a += cp[i * 16];
    scs[e] = a;
  }

  const int r32 = lane & 31, h = lane >> 5, g = lane >> 4, q4 = (lane & 15) >> 2, p4 = lane & 3;
  const int wm = w >> 1, wn = w & 1;
  if (loader) {
#pragma unroll
    for (int c = 0; c < 8; ++c) {
      if (c < 7) asm volatile("s_waitcnt vmcnt(10)" ::: "memory");
      else       asm volatile("s_waitcnt vmcnt(0)" ::: "memory");
      __builtin_amdgcn_s_barrier();
      if (c + 2 < 8) K4_DMA(c + 2);
    }
    return;
  }
  f32x16 acc[2][3] = {};
#pragma unroll
  for (int c = 0; c < 8; ++c) {
    asm volatile("s_waitcnt lgkmcnt(0)" ::: "memory");
    __builtin_amdgcn_s_barrier();
    if (c == 0) TS(3, 1);
    const char* sA = smem + (c % 3) * K4_ST;
    const char* sB = sA + 16384;
#pragma unroll
    for (int s = 0; s < 4; ++s) {
      bf16x8 af[2], bf[3];
#pragma unroll
      for (int i = 0; i < 2; ++i) {
        const int arow = wm * 64 + i * 32 + r32;
        af[i] = *(const bf16x8*)(sA + arow * 128 + (((2 * s + h) ^ ((arow >> 1) & 7)) << 4));
      }
#pragma unroll
      for (int j = 0; j < 3; ++j) bf[j] = *(const bf16x8*)(sB + ((wn * 3 + j) * 4 + s) * 1024 + lane * 16);
#pragma unroll
      for (int i = 0; i < 2; ++i)
#pragma unroll
        for (int j = 0; j < 3; ++j)
          acc[i][j] = __builtin_amdgcn_mfma_f32_32x32x16_bf16(bf[j], af[i], acc[i][j], 0, 0, 0);
    }
  }
  TS(3, 2);
#pragma unroll
  for (int i2 = 0; i2 < 2; ++i2) {
    const int arow = wm * 64 + i2 * 32 + r32;
    const float mean = sstat[arow * 2], rstd = sstat[arow * 2 + 1];
#pragma unroll
    for (int j = 0; j < 3; ++j) {
      if (wn * 3 + j < cnt) {
        const int fc = f0 + (wn * 3 + j) * 16 + 4 * h;
        const float* cp = scs + (wn * 3 + j) * 32 + 4 * h;
        const f32x4 g0 = *(const f32x4*)(cp), g1 = *(const f32x4*)(cp + 8);
        const f32x4 u0 = *(const f32x4*)(cp + 16), u1 = *(const f32x4*)(cp + 24);
        short* go = (short*)(ws + WS_G) + (size_t)(m0 + arow) * FF + fc;
        float r[8];
#pragma unroll
        for (int i = 0; i < 8; ++i) {
          const float gt = (acc[i2][j][i] - mean * (i < 4 ? g0[i & 3] : g1[i & 3])) * rstd;
          const float up = (acc[i2][j][i + 8] - mean * (i < 4 ? u0[i & 3] : u1[i & 3])) * rstd;
          r[i] = gt * __builtin_amdgcn_rcpf(1.f + __expf(-gt)) * up;
        }
        u32x2 a, c;
        a[0] = cvtpk(r[0], r[1]); a[1] = cvtpk(r[2], r[3]);
        c[0] = cvtpk(r[4], r[5]); c[1] = cvtpk(r[6], r[7]);
        *(u32x4*)(go + 4 * h) = widen16(a, c);
      }
    }
  }
  TS(3, 3);
#undef K4_DMA
}

extern "C" void kernel_launch(void* const* d_in, const int* in_sizes, int n_in, void* d_out, int out_size,
                              void* d_ws, size_t ws_size, hipStream_t stream) {
  const short* x = (const short*)d_in[0];
  const short* wq = (const short*)d_in[1];
  const short* wk = (const short*)d_in[2];
  const short* wv = (const short*)d_in[3];
  const short* wo = (const short*)d_in[4];
  const short* w1 = (const short*)d_in[6];
  const short* w3 = (const short*)d_in[7];
  const short* w2 = (const short*)d_in[8];
  char* ws = (char*)d_ws;
  (void)hipFuncSetAttribute((const void*)k1_ln_qkv, hipFuncAttributeMaxDynamicSharedMemorySize, K1_LDS);
  (void)hipFuncSetAttribute((const void*)k2_attn, hipFuncAttributeMaxDynamicSharedMemorySize, ATT_LDS);
  (void)hipFuncSetAttribute((const void*)k_gemm64<DM, true>, hipFuncAttributeMaxDynamicSharedMemorySize, KG_LDS);
  (void)hipFuncSetAttribute((const void*)k4_ffn1, hipFuncAttributeMaxDynamicSharedMemorySize, K4_LDS);
  (void)hipFuncSetAttribute((const void*)k_gemm64<FF, false>, hipFuncAttributeMaxDynamicSharedMemorySize, KG_LDS);
  k1_ln_qkv<<<256, 512, K1_LDS, stream>>>(x, wq, wk, wv, ws);
  k2_attn<<<256, 1024, ATT_LDS, stream>>>(w1, w3, wo, w2, ws);
  k_gemm64<DM, true><<<256, 512, KG_LDS, stream>>>((const short*)(ws + WS_O), ws + WS_WOF, x, (short*)(ws + WS_H), ws);
  k4_ffn1<<<256, 512, K4_LDS, stream>>>(ws);
  k_gemm64<FF, false><<<256, 512, KG_LDS, stream>>>((const short*)(ws + WS_G), ws + WS_W2F, (const short*)(ws + WS_H), (short*)d_out, ws);
}
```

```cpp
#include <hip/hip_runtime.h>
#include <hip/hip_bf16.h>

typedef __attribute__((ext_vector_type(8))) short bf16x8;
typedef __attribute__((ext_vector_type(4))) short s16x4;
typedef __attribute__((ext_vector_type(16))) float f32x16;
typedef __attribute__((ext_vector_type(4))) float f32x4;
typedef __attribute__((ext_vector_type(2))) float f32x2;
typedef __attribute__((ext_vector_type(4))) unsigned u32x4;
typedef __attribute__((ext_vector_type(2))) unsigned u32x2;
typedef __attribute__((ext_vector_type(2))) __bf16 bf16x2_t;
typedef __attribute__((address_space(3))) s16x4 lds_s16x4;

#define DI __device__ __forceinline__

constexpr int S_LEN = 2048, DM = 512, NH = 32, DH = 16, FF = 1376;
constexpr float QSCALE = 0.25f * 1.4426950408889634f;

constexpr size_t WS_Q = 0;
constexpr size_t WS_KF = WS_Q + (size_t)S_LEN * DM * 2;
constexpr size_t WS_VF = WS_KF + (size_t)S_LEN * DH * 2;
constexpr size_t WS_O = WS_VF + (size_t)S_LEN * DH * 2;
constexpr size_t WS_H = WS_O + (size_t)S_LEN * DM * 2;
constexpr size_t WS_G = WS_H + (size_t)S_LEN * DM * 2;
constexpr size_t WS_KM = WS_G + (size_t)S_LEN * FF * 2;
constexpr size_t WS_ST = WS_KM + 512;
constexpr size_t WS_CS = WS_ST + (size_t)S_LEN * 32 * 4;
constexpr size_t WS_WOF = WS_CS + 172 * 128 * 4;
constexpr size_t WS_W13F = WS_WOF + 512 * 1024;
constexpr size_t WS_W2F = WS_W13F + 2752 * 1024;

DI unsigned cvtpk(float lo, float hi) {
  const f32x2 v = {lo, hi};
  return __builtin_bit_cast(unsigned, __builtin_convertvector(v, bf16x2_t));
}
DI float bf2f(short v) { return __uint_as_float(((unsigned)(unsigned short)v) << 16); }
DI float bflo(unsigned u) { return __uint_as_float(u << 16); }
DI float bfhi(unsigned u) { return __uint_as_float(u & 0xffff0000u); }
DI s16x4 tr_read(const char* p) {
  return __builtin_amdgcn_ds_read_tr16_b64_v4i16((lds_s16x4*)p);
}
DI bf16x8 cat8(s16x4 a, s16x4 b) { return __builtin_shufflevector(a, b, 0, 1, 2, 3, 4, 5, 6, 7); }
DI void stat2(unsigned u, float& s, float& q) {
  const float a = bflo(u), b = bfhi(u);
  s += a + b;
  q = fmaf(a, a, fmaf(b, b, q));
}
DI void stat8(const bf16x8& v, float& s, float& q) {
  const u32x4 u = __builtin_bit_cast(u32x4, v);
#pragma unroll
  for (int j = 0; j < 4; ++j) stat2(u[j], s, q);
}
typedef __attribute__((address_space(3))) void lvoid_t;
DI void glds16(const void* gsrc, unsigned lds_dst) {
  unsigned keep;
  asm volatile("s_mov_b32 %0, m0\n\ts_mov_b32 m0, %2\n\ts_nop 0\n\tglobal_load_lds_dwordx4 %1, off\n\ts_mov_b32 m0, %0"
               : "=&s"(keep) : "v"(gsrc), "s"(lds_dst) : "memory");
}
DI void st16_wt(void* p, u32x4 v) {
  asm volatile("global_store_dwordx4 %0, %1, off sc1\n\ts_nop 1" :: "v"(p), "v"(v) : "memory");
}
DI u32x4 widen16(u32x2 a, u32x2 b) {
  const auto r0 = __builtin_amdgcn_permlane32_swap(a[0], b[0], false, false);
  const auto r1 = __builtin_amdgcn_permlane32_swap(a[1], b[1], false, false);
  return u32x4{r0[0], r1[0], r0[1], r1[1]};
}
#define TS(kid, i)

constexpr int PD = 4;

constexpr int K1_ST = 36864;
constexpr int K1_LDS = 4 * K1_ST + 1024;
__global__ __launch_bounds__(512) void k1_ln_qkv(const short* __restrict__ x, const short* __restrict__ wq,
                                                 const short* __restrict__ wk, const short* __restrict__ wv,
                                                 char* __restrict__ ws) {
  extern __shared__ __attribute__((aligned(16))) char smem[];
  float* sstat = (float*)(smem + 4 * K1_ST);
  const int tid = threadIdx.x, lane = tid & 63, w = __builtin_amdgcn_readfirstlane(tid >> 6);
  const int b = blockIdx.x, xcd = b & 7, idx = b >> 3;
  const int mt = xcd * 4 + (idx & 3), nt = idx >> 2;
  const int m0 = mt * 64, n0 = nt * 64;
  const int ar = tid >> 4, ac = tid & 15;
  const short* wkv = (nt & 1) ? wv : wk;
  TS(0, 0);
  {
    const unsigned lbase = (unsigned)(size_t)(lvoid_t*)smem;
    const short* asrc[2];
    const short* bsrc[2];
#pragma unroll
    for (int i = 0; i < 2; ++i) {
      const int row = (w * 2 + i) * 4 + (lane >> 4);
      asrc[i] = x + (size_t)(m0 + row) * DM + (((lane & 15) ^ (row & 15)) << 3);
      const int k = (w * 2 + i) * 8 + (lane >> 3);
      bsrc[i] = wq + (size_t)k * DM + n0 + (((lane & 7) ^ (((k >> 1) & 1) << 2)) << 3);
    }
    const short* csrc = wkv + ((w & 3) * 64 + lane) * 8;
#pragma unroll
    for (int c = 0; c < 4; ++c) {
      const unsigned st_ = lbase + c * K1_ST;
#pragma unroll
      for (int i = 0; i < 2; ++i) {
        glds16(asrc[i] + c * 128, st_ + (w * 2 + i) * 1024);
        glds16(bsrc[i] + (size_t)c * 128 * DM, st_ + 16384 + (w * 2 + i) * 1024);
      }
      glds16(csrc + c * 2048, st_ + 32768 + (w & 3) * 1024);
    }
  }
  float ssum[2] = {0.f, 0.f}, ssq[2] = {0.f, 0.f};

  const int r32 = lane & 31, h = lane >> 5, g = lane >> 4, q4 = (lane & 15) >> 2, p4 = lane & 3;
  const int wq_ = w & 3, kp = w >> 2;
  const int wm = wq_ >> 1, wn = wq_ & 1;
  const int arow = wm * 32 + r32;
  const int nb = (wn * 32 + (g & 1) * 16 + 4 * p4) * 2;
  const int rb = (nt >> 1) * 16;
  bf16x8 ones;
#pragma unroll
  for (int j = 0; j < 8; ++j) ones[j] = (short)0x3F80;
  f32x16 acc = {}, cs = {};
  f32x4 c4 = {}, cs4 = {};
#pragma unroll
  for (int c = 0; c < 4; ++c) {
    if (c == 0)      asm volatile("s_waitcnt vmcnt(15)" ::: "memory");
    else if (c == 1) asm volatile("s_waitcnt vmcnt(10)" ::: "memory");
    else if (c == 2) asm volatile("s_waitcnt vmcnt(5)" ::: "memory");
    else             asm volatile("s_waitcnt vmcnt(0)" ::: "memory");
    __builtin_amdgcn_s_barrier();
    if (c == 0) TS(0, 1);
    const char* sA = smem + c * K1_ST;
    const char* sB = sA + 16384;
    bf16x8 af[4], bf[4];
#pragma unroll
    for (int ss = 0; ss < 4; ++ss) {
      const int s = 2 * ss + kp;
      af[ss] = *(const bf16x8*)(sA + arow * 256 + (((2 * s + h) ^ (arow & 15)) << 4));
      const int k0 = 16 * s + 8 * h + q4;
      bf[ss] = cat8(tr_read(sB + k0 * 128 + (nb ^ (((k0 >> 1) & 1) << 6))),
                    tr_read(sB + (k0 + 4) * 128 + (nb ^ ((((k0 + 4) >> 1) & 1) << 6))));
    }
#pragma unroll
    for (int i = 0; i < 2; ++i) {
      const int row = i * 32 + ar;
      stat8(*(const bf16x8*)(sA + row * 256 + ((ac ^ (row & 15)) << 4)), ssum[i], ssq[i]);
    }
#pragma unroll
    for (int ss = 0; ss < 4; ++ss) {
      acc = __builtin_amdgcn_mfma_f32_32x32x16_bf16(bf[ss], af[ss], acc, 0, 0, 0);
      cs = __builtin_amdgcn_mfma_f32_32x32x16_bf16(bf[ss], ones, cs, 0, 0, 0);
    }
    if (w == 5) {
      const char* sC = sA + 32768;
      const int krow = rb + (lane & 15);
#pragma unroll
      for (int s = 0; s < 4; ++s) {
        const bf16x8 a16 = *(const bf16x8*)(sA + krow * 256 + (((4 * s + g) ^ (krow & 15)) << 4));
        const int k0 = 32 * s + 8 * g + q4;
        const bf16x8 b16 = cat8(tr_read(sC + k0 * 32 + 8 * p4), tr_read(sC + (k0 + 4) * 32 + 8 * p4));
        if (nt & 1) {
          c4 = __builtin_amdgcn_mfma_f32_16x16x32_bf16(a16, b16, c4, 0, 0, 0);
          cs4 = __builtin_amdgcn_mfma_f32_16x16x32_bf16(ones, b16, cs4, 0, 0, 0);
        } else {
          c4 = __builtin_amdgcn_mfma_f32_16x16x32_bf16(b16, a16, c4, 0, 0, 0);
          cs4 = __builtin_amdgcn_mfma_f32_16x16x32_bf16(b16, ones, cs4, 0, 0, 0);
        }
      }
    }
  }
  TS(0, 2);
#pragma unroll
  for (int i = 0; i < 2; ++i) {
    float s_ = ssum[i], q_ = ssq[i];
#pragma unroll
    for (int o = 1; o < 16; o <<= 1) { s_ += __shfl_xor(s_, o); q_ += __shfl_xor(q_, o); }
    if (ac == 0) {
      const float mean = s_ * (1.f / DM);
      const float var = fmaxf(q_ * (1.f / DM) - mean * mean, 0.f);
      sstat[(i * 32 + ar) * 2] = mean;
      sstat[(i * 32 + ar) * 2 + 1] = rsqrtf(var + 1e-5f);
    }
  }
  __syncthreads();
  const float mean = sstat[arow * 2], rstd = sstat[arow * 2 + 1];
#pragma unroll
  for (int i = 0; i < 16; ++i) acc[i] = fmaf(-mean, cs[i], acc[i]);
  float* red = (float*)smem + wq_ * 1024 + lane;
  if (kp == 1) {
#pragma unroll
    for (int i = 0; i < 16; ++i) red[i * 64] = acc[i];
  }
  if (w == 5) {
    float r[4];
    u32x2 o;
    if (nt & 1) {
#pragma unroll
      for (int i = 0; i < 4; ++i) {
        const int rr = rb + 4 * g + i;
        r[i] = (c4[i] - sstat[rr * 2] * cs4[i]) * sstat[rr * 2 + 1];
      }
      o[0] = cvtpk(r[0], r[1]); o[1] = cvtpk(r[2], r[3]);
      const int key0 = m0 + rb;
      const int t = key0 >> 5, s = (key0 >> 4) & 1;
      short* vf = (short*)(ws + WS_VF) + (size_t)t * 512 + s * 256 + (g & 1) * 128 + (lane & 15) * 8 + (g >> 1) * 4;
      *(u32x2*)vf = o;
    } else {
      const int rr = rb + (lane & 15);
      const float mn = sstat[rr * 2], rs = sstat[rr * 2 + 1];
#pragma unroll
      for (int i = 0; i < 4; ++i) r[i] = (c4[i] - mn * cs4[i]) * rs;
      o[0] = cvtpk(r[0], r[1]); o[1] = cvtpk(r[2], r[3]);
      const int key = m0 + rr;
      const int t = key >> 5;
      short* kf = (short*)(ws + WS_KF) + (size_t)t * 512 + (g >> 1) * 256 + (key & 31) * 8 + (g & 1) * 4;
      *(u32x2*)kf = o;
      const float k0 = bflo(o[0]), k1 = bfhi(o[0]), k2 = bflo(o[1]), k3 = bfhi(o[1]);
      float a = k0 * k0 + k1 * k1 + k2 * k2 + k3 * k3;
      a += __shfl_xor(a, 16);
      a += __shfl_xor(a, 32);
#pragma unroll
      for (int of = 8; of > 0; of >>= 1) a = fmaxf(a, __shfl_xor(a, of));
      if (lane == 0) ((float*)(ws + WS_KM))[mt * 4 + (nt >> 1)] = a;
    }
  }
  __syncthreads();
  if (kp == 1) return;
  const float sc = rstd * QSCALE;
  short* qo = (short*)(ws + WS_Q) + (size_t)(m0 + arow) * DM + n0 + wn * 32 + 4 * h;
#pragma unroll
  for (int gp = 0; gp < 2; ++gp) {
    u32x2 o[2];
#pragma unroll
    for (int e = 0; e < 2; ++e) {
      const int gg = 2 * gp + e;
      o[e][0] = cvtpk((acc[4 * gg] + red[(4 * gg) * 64]) * sc, (acc[4 * gg + 1] + red[(4 * gg + 1) * 64]) * sc);
      o[e][1] = cvtpk((acc[4 * gg + 2] + red[(4 * gg + 2) * 64]) * sc, (acc[4 * gg + 3] + red[(4 * gg + 3) * 64]) * sc);
    }
    *(u32x4*)(qo + 4 * h + 16 * gp) = widen16(o[0], o[1]);
  }
  TS(0, 3);
}

constexpr int TS = 2080;
constexpr int ATT_RED = 64 * TS;
constexpr int ATT_LDS = ATT_RED + 8 * 64 * 9 * 4 + 8 * 1024;

__global__ __launch_bounds__(1024) void k2_attn(const short* __restrict__ w1, const short* __restrict__ w3,
                                                const short* __restrict__ wo, const short* __restrict__ w2,
                                                char* __restrict__ ws) {
  extern __shared__ __attribute__((aligned(16))) char smem[];
  const int tid = threadIdx.x, lane = tid & 63, w = tid >> 6;
  const int r32 = lane & 31, h = lane >> 5;
  float* scomb = (float*)(smem + ATT_RED);
  TS(1, 0);
  const int wr = w & 7, kh = w >> 3;
  const int pblk = (blockIdx.x & 7) * 32 + (blockIdx.x >> 3);
  const size_t row = (size_t)pblk * 256 + wr * 32 + r32;
  const int tl0 = (w < 8) ? w : 24 + w;
  bf16x8 kv[8];
  {
    const bf16x8* srck = (const bf16x8*)(ws + WS_KF) + tl0 * 64 + lane;
    const bf16x8* srcv = (const bf16x8*)(ws + WS_VF) + tl0 * 64 + lane;
#pragma unroll
    for (int j = 0; j < 4; ++j) { kv[2 * j] = srck[j * 512]; kv[2 * j + 1] = srcv[j * 512]; }
  }
  const bf16x8 qf = *(const bf16x8*)((const short*)(ws + WS_Q) + row * 16 + h * 8);
  float kn2 = fmaxf(((const float*)(ws + WS_KM))[lane], ((const float*)(ws + WS_KM))[64 + lane]);
  bf16x8 csv0, csv1, rl;
  if (tid < 512) *(unsigned*)(smem + (tid >> 3) * TS + 2048 + (tid & 7) * 4) = (tid & 7) < 4 ? 0u : 0x3F803F80u;
  char* sdst = smem + tl0 * TS + lane * 16;
#pragma unroll
  for (int j = 0; j < 4; ++j) {
    *(bf16x8*)(sdst + j * 8 * TS) = kv[2 * j];
    *(bf16x8*)(sdst + j * 8 * TS + 1024) = kv[2 * j + 1];
  }
#pragma unroll
  for (int o = 32; o > 0; o >>= 1) kn2 = fmaxf(kn2, __shfl_xor(kn2, o));
  float qn2 = 0.f;
#pragma unroll
  for (int j = 0; j < 8; ++j) { const float f = bf2f(qf[j]); qn2 += f * f; }
  qn2 += __shfl_xor(qn2, 32);
  const float mref = sqrtf(qn2 * kn2) * 1.001f;

  const bool vlane = r32 < 16;
  const int kbase = lane * 16;
  const int vbase0 = vlane ? (1024 + h * 256 + r32 * 16) : (2048 + (r32 == 16 ? 16 : 0));
  const int vbase1 = vlane ? vbase0 + 512 : vbase0;
  f32x16 o = {};
  {
    f32x16 negm;
#pragma unroll
    for (int i = 0; i < 16; ++i) negm[i] = -mref;
    const char* kp = smem + kbase + kh * 32 * TS;
    const char* vp0 = smem + vbase0 + kh * 32 * TS;
    const char* vp1 = smem + vbase1 + kh * 32 * TS;
#pragma unroll
    for (int j = 0; j < 4; ++j) {
      if (j == 0) __syncthreads();
      TS(1, 1 + j);
      if (j == 0) {
  {
    const int gi = blockIdx.x < 172 ? blockIdx.x : 0, t2 = tid & 511;
    const short* wsrc = ((gi < 86) ? (w1 + gi * 16) : (w3 + (gi - 86) * 16)) + (size_t)(t2 >> 1) * FF + (t2 & 1) * 8;
    csv0 = *(const bf16x8*)(wsrc);
    csv1 = *(const bf16x8*)(wsrc + (size_t)256 * FF);
  }
      }
      if (j >= 1 && j <= 2) {
        const int u = blockIdx.x * 16 + w + (j - 1) * 4096;
        if (u < 4640) {
          char* scr = (char*)scomb + (kh ? wr * 2304 : 18432 + wr * 1024);
          *(bf16x8*)(scr + (lane >> 2) * 64 + (lane & 3) * 16) = rl;
          const int g4 = lane >> 4;
          const char* rp = scr + (8 * (g4 >> 1) + ((lane & 15) >> 2)) * 64 + ((g4 & 1) * 16 + 4 * (lane & 3)) * 2;
          char* dst = ws + WS_WOF + (size_t)u * 1024;
          st16_wt(dst + lane * 16, __builtin_bit_cast(u32x4, cat8(tr_read(rp), tr_read(rp + 256))));
        }
      }
      if (j <= 1) {
        int u = blockIdx.x * 16 + w + j * 4096;
        u = u < 4640 ? u : 0;
        const int kk = lane >> 2, pc = lane & 3;
        const short* src;
        if (u < 512) {
          src = wo + (size_t)(16 * (u & 31) + kk) * DM + 32 * (u >> 5) + pc * 8;
        } else if (u < 3264) {
          const int v = u - 512;
          src = (pc < 2 ? w1 : w3) + (size_t)(16 * (v & 31) + kk) * FF + (v >> 5) * 16 + (pc & 1) * 8;
        } else {
          const int v = u - 3264;
          src = w2 + (size_t)(16 * (v % 86) + kk) * DM + 32 * (v / 86) + pc * 8;
        }
        rl = *(const bf16x8*)src;
      }
#pragma unroll
      for (int t = j * 8; t < j * 8 + 8; ++t) {
        const bf16x8 kf = *(const bf16x8*)(kp + t * TS);
        const bf16x8 vf0 = *(const bf16x8*)(vp0 + t * TS);
        const bf16x8 vf1 = *(const bf16x8*)(vp1 + t * TS);
        const f32x16 s = __builtin_amdgcn_mfma_f32_32x32x16_bf16(kf, qf, negm, 0, 0, 0);
        u32x4 p0, p1;
#pragma unroll
        for (int i = 0; i < 4; ++i) {
          p0[i] = cvtpk(__builtin_amdgcn_exp2f(s[2 * i]), __builtin_amdgcn_exp2f(s[2 * i + 1]));
          p1[i] = cvtpk(__builtin_amdgcn_exp2f(s[8 + 2 * i]), __builtin_amdgcn_exp2f(s[8 + 2 * i + 1]));
        }
        o = __builtin_amdgcn_mfma_f32_32x32x16_bf16(vf0, __builtin_bit_cast(bf16x8, p0), o, 0, 0, 0);
        o = __builtin_amdgcn_mfma_f32_32x32x16_bf16(vf1, __builtin_bit_cast(bf16x8, p1), o, 0, 0, 0);
      }
    }
  }
  TS(1, 5);
  TS(1, 6);
  float* cb = scomb + wr * (64 * 9) + lane * 9;
  if (kh == 1) {
#pragma unroll
    for (int i = 0; i < 9; ++i) cb[i] = o[i];
  }
  __syncthreads();
  if (kh == 1) {
    if (blockIdx.x < 172) {
      float cf[8];
#pragma unroll
      for (int j = 0; j < 8; ++j) {
        cf[j] = bf2f(csv0[j]) + bf2f(csv1[j]);
#pragma unroll
        for (int of = 2; of < 64; of <<= 1) cf[j] += __shfl_xor(cf[j], of);
      }
      if (lane < 2) {
        float* dst = (float*)(ws + WS_CS) + (size_t)(blockIdx.x * 8 + wr) * 16 + lane * 8;
        *(f32x4*)dst = f32x4{cf[0], cf[1], cf[2], cf[3]};
        *(f32x4*)(dst + 4) = f32x4{cf[4], cf[5], cf[6], cf[7]};
      }
    }
    return;
  }
#pragma unroll
  for (int i = 0; i < 9; ++i) o[i] += cb[i];
  float l = __shfl(o[8], r32);
  if (__any(l < 1e-18f)) {
    f32x16 negm = {};
    o = f32x16{};
#pragma unroll 1
    for (int t = 0; t < 64; ++t) {
      const bf16x8 kf = *(const bf16x8*)(smem + kbase + t * TS);
      const bf16x8 vf0 = *(const bf16x8*)(smem + vbase0 + t * TS);
      const bf16x8 vf1 = *(const bf16x8*)(smem + vbase1 + t * TS);
      f32x16 s = __builtin_amdgcn_mfma_f32_32x32x16_bf16(kf, qf, negm, 0, 0, 0);
      float mx = s[0];
#pragma unroll
      for (int i = 1; i < 16; ++i) mx = fmaxf(mx, s[i]);
      float d = fmaxf(mx, __shfl_xor(mx, 32));
      if (t != 0) d = fmaxf(d, 0.f);
      const float sc = (t != 0) ? __builtin_amdgcn_exp2f(-d) : 1.f;
#pragma unroll
      for (int i = 0; i < 16; ++i) { s[i] -= d; negm[i] -= d; o[i] *= sc; }
      u32x4 p0, p1;
#pragma unroll
      for (int i = 0; i < 4; ++i) {
        p0[i] = cvtpk(__builtin_amdgcn_exp2f(s[2 * i]), __builtin_amdgcn_exp2f(s[2 * i + 1]));
        p1[i] = cvtpk(__builtin_amdgcn_exp2f(s[8 + 2 * i]), __builtin_amdgcn_exp2f(s[8 + 2 * i + 1]));
      }
      o = __builtin_amdgcn_mfma_f32_32x32x16_bf16(vf0, __builtin_bit_cast(bf16x8, p0), o, 0, 0, 0);
      o = __builtin_amdgcn_mfma_f32_32x32x16_bf16(vf1, __builtin_bit_cast(bf16x8, p1), o, 0, 0, 0);
    }
    l = __shfl(o[8], r32);
  }
  const float inv = 1.f / l;
  short* op = (short*)(ws + WS_O) + row * 16 + 4 * h;
  u32x2 a, c;
  a[0] = cvtpk(o[0] * inv, o[1] * inv); a[1] = cvtpk(o[2] * inv, o[3] * inv);
  c[0] = cvtpk(o[4] * inv, o[5] * inv); c[1] = cvtpk(o[6] * inv, o[7] * inv);
  *(u32x4*)(op + 4 * h) = widen16(a, c);
  TS(1, 7);
}

constexpr int KG_LDS = 4 * 32768;
template <int KTOT, bool STATS>
__global__ __launch_bounds__(512) void k_gemm64(const short* __restrict__ A, const char* __restrict__ BF,
                                                const short* __restrict__ resid, short* __restrict__ out,
                                                char* __restrict__ ws) {
  extern __shared__ __attribute__((aligned(16))) char smem[];
  constexpr int NCH = (KTOT + 127) / 128;
  constexpr int KLAST = KTOT - (NCH - 1) * 128;
  constexpr int NKS = KTOT / 16;
  constexpr int NST = 4, DD = 3;
  const int tid = threadIdx.x, lane = tid & 63, w = __builtin_amdgcn_readfirstlane(tid >> 6);
  const int b = blockIdx.x, xcd = b & 7, idx = b >> 3;
  const int mt = xcd * 4 + (idx & 3), nt = idx >> 2;
  const int m0 = mt * 64, n0 = nt * 64;
  TS((STATS ? 2 : 4), 0);
  const short* asrc[2];
  const char* bsrc[2];
  int aco[2];
#pragma unroll
  for (int i = 0; i < 2; ++i) {
    const int row = (w * 2 + i) * 4 + (lane >> 4);
    aco[i] = ((lane & 15) ^ (row & 15)) << 3;
    asrc[i] = A + (size_t)(m0 + row) * KTOT;
    const int e = w * 2 + i;
    bsrc[i] = BF + (size_t)((nt * 2 + (e >> 3)) * NKS + (e & 7)) * 1024 + lane * 16;
  }
  const unsigned lbase = (unsigned)(size_t)(lvoid_t*)smem;
#define KG_DMA(c) do { const unsigned st_ = lbase + ((c) % NST) * 32768; _Pragma("unroll") for (int i = 0; i < 2; ++i) { \
      const int ka = (((c) < NCH - 1) || (aco[i] < KLAST)) ? (c) * 128 + aco[i] : 0; \
      glds16(asrc[i] + ka, st_ + (w * 2 + i) * 1024); \
      const int kb = (((c) < NCH - 1) || (((w * 2 + i) & 7) * 16 < KLAST)) ? (c) * 8192 : 0; \
      glds16(bsrc[i] + kb, st_ + 16384 + (w * 2 + i) * 1024); } } while (0)
  const int r32 = lane & 31, h = lane >> 5;
  const int wq_ = w & 3, kp = w >> 2;
  const int wm = wq_ >> 1, wn = wq_ & 1;
  const int arow = wm * 32 + r32;
  const size_t off = (size_t)(m0 + arow) * DM + n0 + wn * 32 + 4 * h;
  s16x4 xr[4];
#pragma unroll
  for (int gg = 0; gg < 4; ++gg) xr[gg] = *(const s16x4*)(resid + off + 8 * gg);
  asm volatile("" ::: "memory");
  KG_DMA(0); KG_DMA(1); KG_DMA(2);
  f32x16 acc = {};
#pragma unroll
  for (int c = 0; c < NCH; ++c) {
    if (c < NCH - 2)       asm volatile("s_waitcnt vmcnt(8)" ::: "memory");
    else if (c == NCH - 2) asm volatile("s_waitcnt vmcnt(4)" ::: "memory");
    else                   asm volatile("s_waitcnt vmcnt(0)" ::: "memory");
    asm volatile("s_waitcnt lgkmcnt(0)" ::: "memory");
    __builtin_amdgcn_s_barrier();
    if (c == 0) TS((STATS ? 2 : 4), 1);
    if (c + DD < NCH) KG_DMA(c + DD);
    const char* sA = smem + (c % NST) * 32768;
    const char* sB = sA + 16384;
#pragma unroll
    for (int ss = 0; ss < 4; ++ss) {
      const int s = 2 * ss + kp;
      if (c < NCH - 1 || 16 * (2 * ss) < KLAST) {
        const bf16x8 af = *(const bf16x8*)(sA + arow * 256 + (((2 * s + h) ^ (arow & 15)) << 4));
        const bf16x8 bf = *(const bf16x8*)(sB + (wn * 8 + s) * 1024 + lane * 16);
        acc = __builtin_amdgcn_mfma_f32_32x32x16_bf16(bf, af, acc, 0, 0, 0);
      }
    }
  }
  TS((STATS ? 2 : 4), 2);
  float* red = (float*)(smem + (NCH % NST) * 32768) + wq_ * 1024 + lane;
  if (kp == 1) {
#pragma unroll
    for (int i = 0; i < 16; ++i) red[i * 64] = acc[i];
  }
  __syncthreads();
  if (kp == 1) return;
  float ssum = 0.f, ssq = 0.f;
#pragma unroll
  for (int gp = 0; gp < 2; ++gp) {
    u32x2 o[2];
#pragma unroll
    for (int e = 0; e < 2; ++e) {
      const int gg = 2 * gp + e;
      o[e][0] = cvtpk(acc[4 * gg] + red[(4 * gg) * 64] + bf2f(xr[gg][0]), acc[4 * gg + 1] + red[(4 * gg + 1) * 64] + bf2f(xr[gg][1]));
      o[e][1] = cvtpk(acc[4 * gg + 2] + red[(4 * gg + 2) * 64] + bf2f(xr[gg][2]), acc[4 * gg + 3] + red[(4 * gg + 3) * 64] + bf2f(xr[gg][3]));
      if (STATS) {
        stat2(o[e][0], ssum, ssq);
        stat2(o[e][1], ssum, ssq);
      }
    }
    *(u32x4*)(out + off + 4 * h + 16 * gp) = widen16(o[0], o[1]);
  }
  if (STATS) {
    ssum += __shfl_xor(ssum, 32);
    ssq += __shfl_xor(ssq, 32);
    if (h == 0) {
      f32x2 st = {ssum, ssq};
      *(f32x2*)((float*)(ws + WS_ST) + (size_t)(m0 + arow) * 32 + (nt * 2 + wn) * 2) = st;
    }
  }
  TS((STATS ? 2 : 4), 3);
#undef KG_DMA
}

constexpr int K4_ST = 40960;
constexpr int K4_LDS = 3 * K4_ST + 1024 + 1024;
__global__ __launch_bounds__(512) void k4_ffn1(char* __restrict__ ws) {
  extern __shared__ __attribute__((aligned(16))) char smem[];
  float* sstat = (float*)(smem + 3 * K4_ST);
  float* scs = sstat + 256;
  const int tid = threadIdx.x, lane = tid & 63, wall = __builtin_amdgcn_readfirstlane(tid >> 6);
  const int w = wall & 3;
  const bool loader = wall >= 4;
  const int b = blockIdx.x, xcd = b & 7, idx = b >> 3;
  const int mt = xcd * 2 + (idx & 1), ng = idx >> 1;
  const int m0 = mt * 128;
  const int cnt = ng < 6 ? 6 : 5;
  const int f0 = (ng * 5 + (ng < 6 ? ng : 6)) * 16;
  const short* H = (const short*)(ws + WS_H);
  TS(3, 0);
  const short* asrc[4];
  const short* bsrc[6];
#pragma unroll
  for (int i = 0; i < 4; ++i) {
    const int row = (w * 4 + i) * 8 + (lane >> 3);
    asrc[i] = H + (size_t)(m0 + row) * DM + (((lane & 7) ^ ((row >> 1) & 7)) << 3);
  }
#pragma unroll
  for (int i = 0; i < 6; ++i) {
    const int e = w * 6 + i, j = e >> 2, ks = e & 3;
    const int ct = (f0 >> 4) + (j < cnt ? j : 0);
    bsrc[i] = (const short*)(ws + WS_W13F + (size_t)(ct * 32 + ks) * 1024 + lane * 16);
  }
  const unsigned lbase = (unsigned)(size_t)(lvoid_t*)smem;
#define K4_DMA(c) do { const unsigned st_ = lbase + ((c) % 3) * K4_ST; \
    _Pragma("unroll") for (int i = 0; i < 4; ++i) glds16(asrc[i] + (c) * 64, st_ + (w * 4 + i) * 1024); \
    _Pragma("unroll") for (int i = 0; i < 6; ++i) glds16(bsrc[i] + (c) * 2048, st_ + 16384 + (w * 6 + i) * 1024); } while (0)
  if (loader) { K4_DMA(0); K4_DMA(1); }
  if (tid < 128) {
    const f32x4* sp = (const f32x4*)((const float*)(ws + WS_ST) + (size_t)(m0 + tid) * 32);
    float s_ = 0.f, q_ = 0.f;
#pragma unroll
    for (int i = 0; i < 8; ++i) { const f32x4 v = sp[i]; s_ += v[0] + v[2]; q_ += v[1] + v[3]; }
    const float mean = s_ * (1.f / DM);
    const float var = fmaxf(q_ * (1.f / DM) - mean * mean, 0.f);
    sstat[tid * 2] = mean;
    sstat[tid * 2 + 1] = rsqrtf(var + 1e-5f);
  }
  if (tid < 192) {
    const int e = tid, ct = e >> 5, isup = (e >> 4) & 1;
    const int gi = isup * 86 + (f0 >> 4) + (ct < cnt ? ct : 0);
    const float* cp = (const float*)(ws + WS_CS) + (size_t)gi * 128 + (e & 15);
    float a = 0.f;
#pragma unroll
    for (int i = 0; i < 8; ++i) a += cp[i * 16];
    scs[e] = a;
  }

  const int r32 = lane & 31, h = lane >> 5, g = lane >> 4, q4 = (lane & 15) >> 2, p4 = lane & 3;
  const int wm = w >> 1, wn = w & 1;
  if (loader) {
#pragma unroll
    for (int c = 0; c < 8; ++c) {
      if (c < 7) asm volatile("s_waitcnt vmcnt(10)" ::: "memory");
      else       asm volatile("s_waitcnt vmcnt(0)" ::: "memory");
      __builtin_amdgcn_s_barrier();
      if (c + 2 < 8) K4_DMA(c + 2);
    }
    return;
  }
  f32x16 acc[2][3] = {};
#pragma unroll
  for (int c = 0; c < 8; ++c) {
    asm volatile("s_waitcnt lgkmcnt(0)" ::: "memory");
    __builtin_amdgcn_s_barrier();
    if (c == 0) TS(3, 1);
    const char* sA = smem + (c % 3) * K4_ST;
    const char* sB = sA + 16384;
#pragma unroll
    for (int s = 0; s < 4; ++s) {
      bf16x8 af[2], bf[3];
#pragma unroll
      for (int i = 0; i < 2; ++i) {
        const int arow = wm * 64 + i * 32 + r32;
        af[i] = *(const bf16x8*)(sA + arow * 128 + (((2 * s + h) ^ ((arow >> 1) & 7)) << 4));
      }
#pragma unroll
      for (int j = 0; j < 3; ++j) bf[j] = *(const bf16x8*)(sB + ((wn * 3 + j) * 4 + s) * 1024 + lane * 16);
#pragma unroll
      for (int i = 0; i < 2; ++i)
#pragma unroll
        for (int j = 0; j < 3; ++j)
          acc[i][j] = __builtin_amdgcn_mfma_f32_32x32x16_bf16(bf[j], af[i], acc[i][j], 0, 0, 0);
    }
  }
  TS(3, 2);
#pragma unroll
  for (int i2 = 0; i2 < 2; ++i2) {
    const int arow = wm * 64 + i2 * 32 + r32;
    const float mean = sstat[arow * 2], rstd = sstat[arow * 2 + 1];
#pragma unroll
    for (int j = 0; j < 3; ++j) {
      if (wn * 3 + j < cnt) {
        const int fc = f0 + (wn * 3 + j) * 16 + 4 * h;
        const float* cp = scs + (wn * 3 + j) * 32 + 4 * h;
        const f32x4 g0 = *(const f32x4*)(cp), g1 = *(const f32x4*)(cp + 8);
        const f32x4 u0 = *(const f32x4*)(cp + 16), u1 = *(const f32x4*)(cp + 24);
        short* go = (short*)(ws + WS_G) + (size_t)(m0 + arow) * FF + fc;
        float r[8];
#pragma unroll
        for (int i = 0; i < 8; ++i) {
          const float gt = (acc[i2][j][i] - mean * (i < 4 ? g0[i & 3] : g1[i & 3])) * rstd;
          const float up = (acc[i2][j][i + 8] - mean * (i < 4 ? u0[i & 3] : u1[i & 3])) * rstd;
          r[i] = gt * __builtin_amdgcn_rcpf(1.f + __expf(-gt)) * up;
        }
        u32x2 a, c;
        a[0] = cvtpk(r[0], r[1]); a[1] = cvtpk(r[2], r[3]);
        c[0] = cvtpk(r[4], r[5]); c[1] = cvtpk(r[6], r[7]);
        *(u32x4*)(go + 4 * h) = widen16(a, c);
      }
    }
  }
  TS(3, 3);
#undef K4_DMA
}

extern "C" void kernel_launch(void* const* d_in, const int* in_sizes, int n_in, void* d_out, int out_size,
                              void* d_ws, size_t ws_size, hipStream_t stream) {
  const short* x = (const short*)d_in[0];
  const short* wq = (const short*)d_in[1];
  const short* wk = (const short*)d_in[2];
  const short* wv = (const short*)d_in[3];
  const short* wo = (const short*)d_in[4];
  const short* w1 = (const short*)d_in[6];
  const short* w3 = (const short*)d_in[7];
  const short* w2 = (const short*)d_in[8];
  char* ws = (char*)d_ws;
  (void)hipFuncSetAttribute((const void*)k1_ln_qkv, hipFuncAttributeMaxDynamicSharedMemorySize, K1_LDS);
  (void)hipFuncSetAttribute((const void*)k2_attn, hipFuncAttributeMaxDynamicSharedMemorySize, ATT_LDS);
  (void)hipFuncSetAttribute((const void*)k_gemm64<DM, true>, hipFuncAttributeMaxDynamicSharedMemorySize, KG_LDS);
  (void)hipFuncSetAttribute((const void*)k4_ffn1, hipFuncAttributeMaxDynamicSharedMemorySize, K4_LDS);
  (void)hipFuncSetAttribute((const void*)k_gemm64<FF, false>, hipFuncAttributeMaxDynamicSharedMemorySize, KG_LDS);
  k1_ln_qkv<<<256, 512, K1_LDS, stream>>>(x, wq, wk, wv, ws);
  k2_attn<<<256, 1024, ATT_LDS, stream>>>(w1, w3, wo, w2, ws);
  k_gemm64<DM, true><<<256, 512, KG_LDS, stream>>>((const short*)(ws + WS_O), ws + WS_WOF, x, (short*)(ws + WS_H), ws);
  k4_ffn1<<<256, 512, K4_LDS, stream>>>(ws);
  k_gemm64<FF, false><<<256, 512, KG_LDS, stream>>>((const short*)(ws + WS_G), ws + WS_W2F, (const short*)(ws + WS_H), (short*)d_out, ws);
}
```

```cpp
#include <hip/hip_runtime.h>
#include <hip/hip_bf16.h>

typedef __attribute__((ext_vector_type(8))) short bf16x8;
typedef __attribute__((ext_vector_type(4))) short s16x4;
typedef __attribute__((ext_vector_type(16))) float f32x16;
typedef __attribute__((ext_vector_type(4))) float f32x4;
typedef __attribute__((ext_vector_type(2))) float f32x2;
typedef __attribute__((ext_vector_type(4))) unsigned u32x4;
typedef __attribute__((ext_vector_type(2))) unsigned u32x2;
typedef __attribute__((ext_vector_type(2))) __bf16 bf16x2_t;
typedef __attribute__((address_space(3))) s16x4 lds_s16x4;

#define DI __device__ __forceinline__

constexpr int S_LEN = 2048, DM = 512, NH = 32, DH = 16, FF = 1376;
constexpr float QSCALE = 0.25f * 1.4426950408889634f;

constexpr size_t WS_Q = 0;
constexpr size_t WS_KF = WS_Q + (size_t)S_LEN * DM * 2;
constexpr size_t WS_VF = WS_KF + (size_t)S_LEN * DH * 2;
constexpr size_t WS_O = WS_VF + (size_t)S_LEN * DH * 2;
constexpr size_t WS_H = WS_O + (size_t)S_LEN * DM * 2;
constexpr size_t WS_G = WS_H + (size_t)S_LEN * DM * 2;
constexpr size_t WS_KM = WS_G + (size_t)S_LEN * FF * 2;
constexpr size_t WS_ST = WS_KM + 512;
constexpr size_t WS_CS = WS_ST + (size_t)S_LEN * 32 * 4;
constexpr size_t WS_WOF = WS_CS + 172 * 128 * 4;
constexpr size_t WS_W13F = WS_WOF + 512 * 1024;
constexpr size_t WS_W2F = WS_W13F + 2752 * 1024;

DI unsigned cvtpk(float lo, float hi) {
  const f32x2 v = {lo, hi};
  return __builtin_bit_cast(unsigned, __builtin_convertvector(v, bf16x2_t));
}
DI float bf2f(short v) { return __uint_as_float(((unsigned)(unsigned short)v) << 16); }
DI float bflo(unsigned u) { return __uint_as_float(u << 16); }
DI float bfhi(unsigned u) { return __uint_as_float(u & 0xffff0000u); }
DI s16x4 tr_read(const char* p) {
  return __builtin_amdgcn_ds_read_tr16_b64_v4i16((lds_s16x4*)p);
}
DI bf16x8 cat8(s16x4 a, s16x4 b) { return __builtin_shufflevector(a, b, 0, 1, 2, 3, 4, 5, 6, 7); }
DI void stat2(unsigned u, float& s, float& q) {
  const float a = bflo(u), b = bfhi(u);
  s += a + b;
  q = fmaf(a, a, fmaf(b, b, q));
}
DI void stat8(const bf16x8& v, float& s, float& q) {
  const u32x4 u = __builtin_bit_cast(u32x4, v);
#pragma unroll
  for (int j = 0; j < 4; ++j) stat2(u[j], s, q);
}
typedef __attribute__((address_space(3))) void lvoid_t;
DI void glds16(const void* gsrc, unsigned lds_dst) {
  unsigned keep;
  asm volatile("s_mov_b32 %0, m0\n\ts_mov_b32 m0, %2\n\ts_nop 0\n\tglobal_load_lds_dwordx4 %1, off\n\ts_mov_b32 m0, %0"
               : "=&s"(keep) : "v"(gsrc), "s"(lds_dst) : "memory");
}
DI void st16_wt(void* p, u32x4 v) {
  asm volatile("global_store_dwordx4 %0, %1, off sc1\n\ts_nop 1" :: "v"(p), "v"(v) : "memory");
}
DI u32x4 widen16(u32x2 a, u32x2 b) {
  const auto r0 = __builtin_amdgcn_permlane32_swap(a[0], b[0], false, false);
  const auto r1 = __builtin_amdgcn_permlane32_swap(a[1], b[1], false, false);
  return u32x4{r0[0], r1[0], r0[1], r1[1]};
}
#define TS(kid, i)

constexpr int PD = 4;

constexpr int K1_ST = 36864;
constexpr int K1_LDS = 2 * K1_ST + 1024;
__global__ __launch_bounds__(512) void k1_ln_qkv(const short* __restrict__ x, const short* __restrict__ wq,
                                                 const short* __restrict__ wk, const short* __restrict__ wv,
                                                 char* __restrict__ ws) {
  extern __shared__ __attribute__((aligned(16))) char smem[];
  float* sstat = (float*)(smem + 2 * K1_ST);
  const int tid = threadIdx.x, lane = tid & 63, w = tid >> 6;
  const int b = blockIdx.x, xcd = b & 7, idx = b >> 3;
  const int mt = xcd * 4 + (idx & 3), nt = idx >> 2;
  const int m0 = mt * 64, n0 = nt * 64;
  const int ar = tid >> 4, ac = tid & 15, bk = tid >> 3, bc = tid & 7;
  const short* wkv = (nt & 1) ? wv : wk;
  TS(0, 0);
  bf16x8 av[PD][2], bv[PD][2], cv[PD];
#define K1_LOAD(c) do { _Pragma("unroll") for (int i = 0; i < 2; ++i) { \
      av[(c) % PD][i] = *(const bf16x8*)(x + (size_t)(m0 + i * 32 + ar) * DM + (c) * 128 + ac * 8); \
      bv[(c) % PD][i] = *(const bf16x8*)(wq + (size_t)((c) * 128 + i * 64 + bk) * DM + n0 + bc * 8); } \
    cv[(c) % PD] = *(const bf16x8*)(wkv + (size_t)(c) * 2048 + (tid & 255) * 8); } while (0)
#define K1_WRITE(c) do { char* sA_ = smem + ((c) & 1) * K1_ST; _Pragma("unroll") for (int i = 0; i < 2; ++i) { \
      const int row = i * 32 + ar; \
      *(bf16x8*)(sA_ + row * 256 + ((ac ^ (row & 15)) << 4)) = av[(c) % PD][i]; \
      stat8(av[(c) % PD][i], ssum[i], ssq[i]); \
      const int k = i * 64 + bk; \
      *(bf16x8*)(sA_ + 16384 + k * 128 + ((bc << 4) ^ (((k >> 1) & 1) << 6))) = bv[(c) % PD][i]; } \
    *(bf16x8*)(sA_ + 32768 + (tid & 255) * 16) = cv[(c) % PD]; } while (0)
  float ssum[2] = {0.f, 0.f}, ssq[2] = {0.f, 0.f};
  K1_LOAD(0); K1_LOAD(1); K1_LOAD(2); K1_LOAD(3);
  asm volatile("" ::: "memory");
  K1_WRITE(0);

  const int r32 = lane & 31, h = lane >> 5, g = lane >> 4, q4 = (lane & 15) >> 2, p4 = lane & 3;
  const int wq_ = w & 3, kp = w >> 2;
  const int wm = wq_ >> 1, wn = wq_ & 1;
  const int arow = wm * 32 + r32;
  const int nb = (wn * 32 + (g & 1) * 16 + 4 * p4) * 2;
  const int rb = (nt >> 1) * 16;
  bf16x8 ones;
#pragma unroll
  for (int j = 0; j < 8; ++j) ones[j] = (short)0x3F80;
  f32x16 acc = {}, cs = {};
  f32x4 c4 = {}, cs4 = {};
#pragma unroll
  for (int c = 0; c < 4; ++c) {
    __syncthreads();
    if (c == 0) TS(0, 1);
    const char* sA = smem + (c & 1) * K1_ST;
    const char* sB = sA + 16384;
    bf16x8 af[4], bf[4];
#pragma unroll
    for (int ss = 0; ss < 4; ++ss) {
      const int s = 2 * ss + kp;
      af[ss] = *(const bf16x8*)(sA + arow * 256 + (((2 * s + h) ^ (arow & 15)) << 4));
      const int k0 = 16 * s + 8 * h + q4;
      bf[ss] = cat8(tr_read(sB + k0 * 128 + (nb ^ (((k0 >> 1) & 1) << 6))),
                    tr_read(sB + (k0 + 4) * 128 + (nb ^ ((((k0 + 4) >> 1) & 1) << 6))));
    }
    if (c < 3) { K1_WRITE(c + 1); }
#pragma unroll
    for (int ss = 0; ss < 4; ++ss) {
      acc = __builtin_amdgcn_mfma_f32_32x32x16_bf16(bf[ss], af[ss], acc, 0, 0, 0);
      cs = __builtin_amdgcn_mfma_f32_32x32x16_bf16(bf[ss], ones, cs, 0, 0, 0);
    }
    if (w == 5) {
      const char* sC = sA + 32768;
      const int krow = rb + (lane & 15);
#pragma unroll
      for (int s = 0; s < 4; ++s) {
        const bf16x8 a16 = *(const bf16x8*)(sA + krow * 256 + (((4 * s + g) ^ (krow & 15)) << 4));
        const int k0 = 32 * s + 8 * g + q4;
        const bf16x8 b16 = cat8(tr_read(sC + k0 * 32 + 8 * p4), tr_read(sC + (k0 + 4) * 32 + 8 * p4));
        if (nt & 1) {
          c4 = __builtin_amdgcn_mfma_f32_16x16x32_bf16(a16, b16, c4, 0, 0, 0);
          cs4 = __builtin_amdgcn_mfma_f32_16x16x32_bf16(ones, b16, cs4, 0, 0, 0);
        } else {
          c4 = __builtin_amdgcn_mfma_f32_16x16x32_bf16(b16, a16, c4, 0, 0, 0);
          cs4 = __builtin_amdgcn_mfma_f32_16x16x32_bf16(b16, ones, cs4, 0, 0, 0);
        }
      }
    }
  }
  TS(0, 2);
#pragma unroll
  for (int i = 0; i < 2; ++i) {
    float s_ = ssum[i], q_ = ssq[i];
#pragma unroll
    for (int o = 1; o < 16; o <<= 1) { s_ += __shfl_xor(s_, o); q_ += __shfl_xor(q_, o); }
    if (ac == 0) {
      const float mean = s_ * (1.f / DM);
      const float var = fmaxf(q_ * (1.f / DM) - mean * mean, 0.f);
      sstat[(i * 32 + ar) * 2] = mean;
      sstat[(i * 32 + ar) * 2 + 1] = rsqrtf(var + 1e-5f);
    }
  }
  __syncthreads();
  const float mean = sstat[arow * 2], rstd = sstat[arow * 2 + 1];
#pragma unroll
  for (int i = 0; i < 16; ++i) acc[i] = fmaf(-mean, cs[i], acc[i]);
  float* red = (float*)smem + wq_ * 1024 + lane;
  if (kp == 1) {
#pragma unroll
    for (int i = 0; i < 16; ++i) red[i * 64] = acc[i];
  }
  if (w == 5) {
    float r[4];
    u32x2 o;
    if (nt & 1) {
#pragma unroll
      for (int i = 0; i < 4; ++i) {
        const int rr = rb + 4 * g + i;
        r[i] = (c4[i] - sstat[rr * 2] * cs4[i]) * sstat[rr * 2 + 1];
      }
      o[0] = cvtpk(r[0], r[1]); o[1] = cvtpk(r[2], r[3]);
      const int key0 = m0 + rb;
      const int t = key0 >> 5, s = (key0 >> 4) & 1;
      short* vf = (short*)(ws + WS_VF) + (size_t)t * 512 + s * 256 + (g & 1) * 128 + (lane & 15) * 8 + (g >> 1) * 4;
      *(u32x2*)vf = o;
    } else {
      const int rr = rb + (lane & 15);
      const float mn = sstat[rr * 2], rs = sstat[rr * 2 + 1];
#pragma unroll
      for (int i = 0; i < 4; ++i) r[i] = (c4[i] - mn * cs4[i]) * rs;
      o[0] = cvtpk(r[0], r[1]); o[1] = cvtpk(r[2], r[3]);
      const int key = m0 + rr;
      const int t = key >> 5;
      short* kf = (short*)(ws + WS_KF) + (size_t)t * 512 + (g >> 1) * 256 + (key & 31) * 8 + (g & 1) * 4;
      *(u32x2*)kf = o;
      const float k0 = bflo(o[0]), k1 = bfhi(o[0]), k2 = bflo(o[1]), k3 = bfhi(o[1]);
      float a = k0 * k0 + k1 * k1 + k2 * k2 + k3 * k3;
      a += __shfl_xor(a, 16);
      a += __shfl_xor(a, 32);
#pragma unroll
      for (int of = 8; of > 0; of >>= 1) a = fmaxf(a, __shfl_xor(a, of));
      if (lane == 0) ((float*)(ws + WS_KM))[mt * 4 + (nt >> 1)] = a;
    }
  }
  __syncthreads();
  if (kp == 1) return;
  const float sc = rstd * QSCALE;
  short* qo = (short*)(ws + WS_Q) + (size_t)(m0 + arow) * DM + n0 + wn * 32 + 4 * h;
#pragma unroll
  for (int gp = 0; gp < 2; ++gp) {
    u32x2 o[2];
#pragma unroll
    for (int e = 0; e < 2; ++e) {
      const int gg = 2 * gp + e;
      o[e][0] = cvtpk((acc[4 * gg] + red[(4 * gg) * 64]) * sc, (acc[4 * gg + 1] + red[(4 * gg + 1) * 64]) * sc);
      o[e][1] = cvtpk((acc[4 * gg + 2] + red[(4 * gg + 2) * 64]) * sc, (acc[4 * gg + 3] + red[(4 * gg + 3) * 64]) * sc);
    }
    *(u32x4*)(qo + 4 * h + 16 * gp) = widen16(o[0], o[1]);
  }
  TS(0, 3);
#undef K1_LOAD
#undef K1_WRITE
}

constexpr int TS = 2080;
constexpr int ATT_RED = 64 * TS;
constexpr int ATT_LDS = ATT_RED + 8 * 64 * 9 * 4 + 8 * 1024;

__global__ __launch_bounds__(1024) void k2_attn(const short* __restrict__ w1, const short* __restrict__ w3,
                                                const short* __restrict__ wo, const short* __restrict__ w2,
                                                char* __restrict__ ws) {
  extern __shared__ __attribute__((aligned(16))) char smem[];
  const int tid = threadIdx.x, lane = tid & 63, w = tid >> 6;
  const int r32 = lane & 31, h = lane >> 5;
  float* scomb = (float*)(smem + ATT_RED);
  TS(1, 0);
  const int wr = w & 7, kh = w >> 3;
  const int pblk = (blockIdx.x & 7) * 32 + (blockIdx.x >> 3);
  const size_t row = (size_t)pblk * 256 + wr * 32 + r32;
  const int tl0 = (w < 8) ? w : 24 + w;
  bf16x8 kv[8];
  {
    const bf16x8* srck = (const bf16x8*)(ws + WS_KF) + tl0 * 64 + lane;
    const bf16x8* srcv = (const bf16x8*)(ws + WS_VF) + tl0 * 64 + lane;
#pragma unroll
    for (int j = 0; j < 4; ++j) { kv[2 * j] = srck[j * 512]; kv[2 * j + 1] = srcv[j * 512]; }
  }
  const bf16x8 qf = *(const bf16x8*)((const short*)(ws + WS_Q) + row * 16 + h * 8);
  float kn2 = fmaxf(((const float*)(ws + WS_KM))[lane], ((const float*)(ws + WS_KM))[64 + lane]);
  bf16x8 csv0, csv1, rl;
  if (tid < 512) *(unsigned*)(smem + (tid >> 3) * TS + 2048 + (tid & 7) * 4) = (tid & 7) < 4 ? 0u : 0x3F803F80u;
  char* sdst = smem + tl0 * TS + lane * 16;
#pragma unroll
  for (int j = 0; j < 4; ++j) {
    *(bf16x8*)(sdst + j * 8 * TS) = kv[2 * j];
    *(bf16x8*)(sdst + j * 8 * TS + 1024) = kv[2 * j + 1];
  }
#pragma unroll
  for (int o = 32; o > 0; o >>= 1) kn2 = fmaxf(kn2, __shfl_xor(kn2, o));
  float qn2 = 0.f;
#pragma unroll
  for (int j = 0; j < 8; ++j) { const float f = bf2f(qf[j]); qn2 += f * f; }
  qn2 += __shfl_xor(qn2, 32);
  const float mref = sqrtf(qn2 * kn2) * 1.001f;

  const bool vlane = r32 < 16;
  const int kbase = lane * 16;
  const int vbase0 = vlane ? (1024 + h * 256 + r32 * 16) : (2048 + (r32 == 16 ? 16 : 0));
  const int vbase1 = vlane ? vbase0 + 512 : vbase0;
  f32x16 o = {};
  {
    f32x16 negm;
#pragma unroll
    for (int i = 0; i < 16; ++i) negm[i] = -mref;
    const char* kp = smem + kbase + kh * 32 * TS;
    const char* vp0 = smem + vbase0 + kh * 32 * TS;
    const char* vp1 = smem + vbase1 + kh * 32 * TS;
#pragma unroll
    for (int j = 0; j < 4; ++j) {
      if (j == 0) __syncthreads();
      TS(1, 1 + j);
      if (j == 0) {
  {
    const int gi = blockIdx.x < 172 ? blockIdx.x : 0, t2 = tid & 511;
    const short* wsrc = ((gi < 86) ? (w1 + gi * 16) : (w3 + (gi - 86) * 16)) + (size_t)(t2 >> 1) * FF + (t2 & 1) * 8;
    csv0 = *(const bf16x8*)(wsrc);
    csv1 = *(const bf16x8*)(wsrc + (size_t)256 * FF);
  }
      }
      if (j >= 1 && j <= 2) {
        const int u = blockIdx.x * 16 + w + (j - 1) * 4096;
        if (u < 4640) {
          char* scr = (char*)scomb + (kh ? wr * 2304 : 18432 + wr * 1024);
          *(bf16x8*)(scr + (lane >> 2) * 64 + (lane & 3) * 16) = rl;
          const int g4 = lane >> 4;
          const char* rp = scr + (8 * (g4 >> 1) + ((lane & 15) >> 2)) * 64 + ((g4 & 1) * 16 + 4 * (lane & 3)) * 2;
          char* dst = ws + WS_WOF + (size_t)u * 1024;
          st16_wt(dst + lane * 16, __builtin_bit_cast(u32x4, cat8(tr_read(rp), tr_read(rp + 256))));
        }
      }
      if (j <= 1) {
        int u = blockIdx.x * 16 + w + j * 4096;
        u = u < 4640 ? u : 0;
        const int kk = lane >> 2, pc = lane & 3;
        const short* src;
        if (u < 512) {
          src = wo + (size_t)(16 * (u & 31) + kk) * DM + 32 * (u >> 5) + pc * 8;
        } else if (u < 3264) {
          const int v = u - 512;
          src = (pc < 2 ? w1 : w3) + (size_t)(16 * (v & 31) + kk) * FF + (v >> 5) * 16 + (pc & 1) * 8;
        } else {
          const int v = u - 3264;
          src = w2 + (size_t)(16 * (v % 86) + kk) * DM + 32 * (v / 86) + pc * 8;
        }
        rl = *(const bf16x8*)src;
      }
#pragma unroll
      for (int t = j * 8; t < j * 8 + 8; ++t) {
        const bf16x8 kf = *(const bf16x8*)(kp + t * TS);
        const bf16x8 vf0 = *(const bf16x8*)(vp0 + t * TS);
        const bf16x8 vf1 = *(const bf16x8*)(vp1 + t * TS);
        const f32x16 s = __builtin_amdgcn_mfma_f32_32x32x16_bf16(kf, qf, negm, 0, 0, 0);
        u32x4 p0, p1;
#pragma unroll
        for (int i = 0; i < 4; ++i) {
          p0[i] = cvtpk(__builtin_amdgcn_exp2f(s[2 * i]), __builtin_amdgcn_exp2f(s[2 * i + 1]));
          p1[i] = cvtpk(__builtin_amdgcn_exp2f(s[8 + 2 * i]), __builtin_amdgcn_exp2f(s[8 + 2 * i + 1]));
        }
        o = __builtin_amdgcn_mfma_f32_32x32x16_bf16(vf0, __builtin_bit_cast(bf16x8, p0), o, 0, 0, 0);
        o = __builtin_amdgcn_mfma_f32_32x32x16_bf16(vf1, __builtin_bit_cast(bf16x8, p1), o, 0, 0, 0);
      }
    }
  }
  TS(1, 5);
  TS(1, 6);
  float* cb = scomb + wr * (64 * 9) + lane * 9;
  if (kh == 1) {
#pragma unroll
    for (int i = 0; i < 9; ++i) cb[i] = o[i];
  }
  __syncthreads();
  if (kh == 1) {
    if (blockIdx.x < 172) {
      float cf[8];
#pragma unroll
      for (int j = 0; j < 8; ++j) {
        cf[j] = bf2f(csv0[j]) + bf2f(csv1[j]);
#pragma unroll
        for (int of = 2; of < 64; of <<= 1) cf[j] += __shfl_xor(cf[j], of);
      }
      if (lane < 2) {
        float* dst = (float*)(ws + WS_CS) + (size_t)(blockIdx.x * 8 + wr) * 16 + lane * 8;
        *(f32x4*)dst = f32x4{cf[0], cf[1], cf[2], cf[3]};
        *(f32x4*)(dst + 4) = f32x4{cf[4], cf[5], cf[6], cf[7]};
      }
    }
    return;
  }
#pragma unroll
  for (int i = 0; i < 9; ++i) o[i] += cb[i];
  float l = __shfl(o[8], r32);
  if (__any(l < 1e-18f)) {
    f32x16 negm = {};
    o = f32x16{};
#pragma unroll 1
    for (int t = 0; t < 64; ++t) {
      const bf16x8 kf = *(const bf16x8*)(smem + kbase + t * TS);
      const bf16x8 vf0 = *(const bf16x8*)(smem + vbase0 + t * TS);
      const bf16x8 vf1 = *(const bf16x8*)(smem + vbase1 + t * TS);
      f32x16 s = __builtin_amdgcn_mfma_f32_32x32x16_bf16(kf, qf, negm, 0, 0, 0);
      float mx = s[0];
#pragma unroll
      for (int i = 1; i < 16; ++i) mx = fmaxf(mx, s[i]);
      float d = fmaxf(mx, __shfl_xor(mx, 32));
      if (t != 0) d = fmaxf(d, 0.f);
      const float sc = (t != 0) ? __builtin_amdgcn_exp2f(-d) : 1.f;
#pragma unroll
      for (int i = 0; i < 16; ++i) { s[i] -= d; negm[i] -= d; o[i] *= sc; }
      u32x4 p0, p1;
#pragma unroll
      for (int i = 0; i < 4; ++i) {
        p0[i] = cvtpk(__builtin_amdgcn_exp2f(s[2 * i]), __builtin_amdgcn_exp2f(s[2 * i + 1]));
        p1[i] = cvtpk(__builtin_amdgcn_exp2f(s[8 + 2 * i]), __builtin_amdgcn_exp2f(s[8 + 2 * i + 1]));
      }
      o = __builtin_amdgcn_mfma_f32_32x32x16_bf16(vf0, __builtin_bit_cast(bf16x8, p0), o, 0, 0, 0);
      o = __builtin_amdgcn_mfma_f32_32x32x16_bf16(vf1, __builtin_bit_cast(bf16x8, p1), o, 0, 0, 0);
    }
    l = __shfl(o[8], r32);
  }
  const float inv = 1.f / l;
  short* op = (short*)(ws + WS_O) + row * 16 + 4 * h;
  u32x2 a, c;
  a[0] = cvtpk(o[0] * inv, o[1] * inv); a[1] = cvtpk(o[2] * inv, o[3] * inv);
  c[0] = cvtpk(o[4] * inv, o[5] * inv); c[1] = cvtpk(o[6] * inv, o[7] * inv);
  *(u32x4*)(op + 4 * h) = widen16(a, c);
  TS(1, 7);
}

constexpr int KG_LDS = 4 * 32768;
template <int KTOT, bool STATS>
__global__ __launch_bounds__(512) void k_gemm64(const short* __restrict__ A, const char* __restrict__ BF,
                                                const short* __restrict__ resid, short* __restrict__ out,
                                                char* __restrict__ ws) {
  extern __shared__ __attribute__((aligned(16))) char smem[];
  constexpr int NCH = (KTOT + 127) / 128;
  constexpr int KLAST = KTOT - (NCH - 1) * 128;
  constexpr int NKS = KTOT / 16;
  constexpr int NST = 4, DD = 3;
  const int tid = threadIdx.x, lane = tid & 63, w = __builtin_amdgcn_readfirstlane(tid >> 6);
  const int b = blockIdx.x, xcd = b & 7, idx = b >> 3;
  const int mt = xcd * 4 + (idx & 3), nt = idx >> 2;
  const int m0 = mt * 64, n0 = nt * 64;
  TS((STATS ? 2 : 4), 0);
  const short* asrc[2];
  const char* bsrc[2];
  int aco[2];
#pragma unroll
  for (int i = 0; i < 2; ++i) {
    const int row = (w * 2 + i) * 4 + (lane >> 4);
    aco[i] = ((lane & 15) ^ (row & 15)) << 3;
    asrc[i] = A + (size_t)(m0 + row) * KTOT;
    const int e = w * 2 + i;
    bsrc[i] = BF + (size_t)((nt * 2 + (e >> 3)) * NKS + (e & 7)) * 1024 + lane * 16;
  }
  const unsigned lbase = (unsigned)(size_t)(lvoid_t*)smem;
#define KG_DMA(c) do { const unsigned st_ = lbase + ((c) % NST) * 32768; _Pragma("unroll") for (int i = 0; i < 2; ++i) { \
      const int ka = (((c) < NCH - 1) || (aco[i] < KLAST)) ? (c) * 128 + aco[i] : 0; \
      glds16(asrc[i] + ka, st_ + (w * 2 + i) * 1024); \
      const int kb = (((c) < NCH - 1) || (((w * 2 + i) & 7) * 16 < KLAST)) ? (c) * 8192 : 0; \
      glds16(bsrc[i] + kb, st_ + 16384 + (w * 2 + i) * 1024); } } while (0)
  const int r32 = lane & 31, h = lane >> 5;
  const int wq_ = w & 3, kp = w >> 2;
  const int wm = wq_ >> 1, wn = wq_ & 1;
  const int arow = wm * 32 + r32;
  const size_t off = (size_t)(m0 + arow) * DM + n0 + wn * 32 + 4 * h;
  s16x4 xr[4];
#pragma unroll
  for (int gg = 0; gg < 4; ++gg) xr[gg] = *(const s16x4*)(resid + off + 8 * gg);
  asm volatile("" ::: "memory");
  KG_DMA(0); KG_DMA(1); KG_DMA(2);
  f32x16 acc = {};
#pragma unroll
  for (int c = 0; c < NCH; ++c) {
    if (c < NCH - 2)       asm volatile("s_waitcnt vmcnt(8)" ::: "memory");
    else if (c == NCH - 2) asm volatile("s_waitcnt vmcnt(4)" ::: "memory");
    else                   asm volatile("s_waitcnt vmcnt(0)" ::: "memory");
    asm volatile("s_waitcnt lgkmcnt(0)" ::: "memory");
    __builtin_amdgcn_s_barrier();
    if (c == 0) TS((STATS ? 2 : 4), 1);
    if (c + DD < NCH) KG_DMA(c + DD);
    const char* sA = smem + (c % NST) * 32768;
    const char* sB = sA + 16384;
#pragma unroll
    for (int ss = 0; ss < 4; ++ss) {
      const int s = 2 * ss + kp;
      if (c < NCH - 1 || 16 * (2 * ss) < KLAST) {
        const bf16x8 af = *(const bf16x8*)(sA + arow * 256 + (((2 * s + h) ^ (arow & 15)) << 4));
        const bf16x8 bf = *(const bf16x8*)(sB + (wn * 8 + s) * 1024 + lane * 16);
        acc = __builtin_amdgcn_mfma_f32_32x32x16_bf16(bf, af, acc, 0, 0, 0);
      }
    }
  }
  TS((STATS ? 2 : 4), 2);
  float* red = (float*)(smem + (NCH % NST) * 32768) + wq_ * 1024 + lane;
  if (kp == 1) {
#pragma unroll
    for (int i = 0; i < 16; ++i) red[i * 64] = acc[i];
  }
  __syncthreads();
  if (kp == 1) return;
  float ssum = 0.f, ssq = 0.f;
#pragma unroll
  for (int gp = 0; gp < 2; ++gp) {
    u32x2 o[2];
#pragma unroll
    for (int e = 0; e < 2; ++e) {
      const int gg = 2 * gp + e;
      o[e][0] = cvtpk(acc[4 * gg] + red[(4 * gg) * 64] + bf2f(xr[gg][0]), acc[4 * gg + 1] + red[(4 * gg + 1) * 64] + bf2f(xr[gg][1]));
      o[e][1] = cvtpk(acc[4 * gg + 2] + red[(4 * gg + 2) * 64] + bf2f(xr[gg][2]), acc[4 * gg + 3] + red[(4 * gg + 3) * 64] + bf2f(xr[gg][3]));
      if (STATS) {
        stat2(o[e][0], ssum, ssq);
        stat2(o[e][1], ssum, ssq);
      }
    }
    *(u32x4*)(out + off + 4 * h + 16 * gp) = widen16(o[0], o[1]);
  }
  if (STATS) {
    ssum += __shfl_xor(ssum, 32);
    ssq += __shfl_xor(ssq, 32);
    if (h == 0) {
      f32x2 st = {ssum, ssq};
      *(f32x2*)((float*)(ws + WS_ST) + (size_t)(m0 + arow) * 32 + (nt * 2 + wn) * 2) = st;
    }
  }
  TS((STATS ? 2 : 4), 3);
#undef KG_DMA
}

constexpr int K4_ST = 40960;
constexpr int K4_LDS = 3 * K4_ST + 1024 + 1024;
__global__ __launch_bounds__(512) void k4_ffn1(char* __restrict__ ws) {
  extern __shared__ __attribute__((aligned(16))) char smem[];
  float* sstat = (float*)(smem + 3 * K4_ST);
  float* scs = sstat + 256;
  const int tid = threadIdx.x, lane = tid & 63, wall = __builtin_amdgcn_readfirstlane(tid >> 6);
  const int w = wall & 3;
  const bool loader = wall >= 4;
  const int b = blockIdx.x, xcd = b & 7, idx = b >> 3;
  const int mt = xcd * 2 + (idx & 1), ng = idx >> 1;
  const int m0 = mt * 128;
  const int cnt = ng < 6 ? 6 : 5;
  const int f0 = (ng * 5 + (ng < 6 ? ng : 6)) * 16;
  const short* H = (const short*)(ws + WS_H);
  TS(3, 0);
  const short* asrc[4];
  const short* bsrc[6];
#pragma unroll
  for (int i = 0; i < 4; ++i) {
    const int row = (w * 4 + i) * 8 + (lane >> 3);
    asrc[i] = H + (size_t)(m0 + row) * DM + (((lane & 7) ^ ((row >> 1) & 7)) << 3);
  }
#pragma unroll
  for (int i = 0; i < 6; ++i) {
    const int e = w * 6 + i, j = e >> 2, ks = e & 3;
    const int ct = (f0 >> 4) + (j < cnt ? j : 0);
    bsrc[i] = (const short*)(ws + WS_W13F + (size_t)(ct * 32 + ks) * 1024 + lane * 16);
  }
  const unsigned lbase = (unsigned)(size_t)(lvoid_t*)smem;
#define K4_DMA(c) do { const unsigned st_ = lbase + ((c) % 3) * K4_ST; \
    _Pragma("unroll") for (int i = 0; i < 4; ++i) glds16(asrc[i] + (c) * 64, st_ + (w * 4 + i) * 1024); \
    _Pragma("unroll") for (int i = 0; i < 6; ++i) glds16(bsrc[i] + (c) * 2048, st_ + 16384 + (w * 6 + i) * 1024); } while (0)
  if (loader) { K4_DMA(0); K4_DMA(1); }
  if (tid < 128) {
    const f32x4* sp = (const f32x4*)((const float*)(ws + WS_ST) + (size_t)(m0 + tid) * 32);
    float s_ = 0.f, q_ = 0.f;
#pragma unroll
    for (int i = 0; i < 8; ++i) { const f32x4 v = sp[i]; s_ += v[0] + v[2]; q_ += v[1] + v[3]; }
    const float mean = s_ * (1.f / DM);
    const float var = fmaxf(q_ * (1.f / DM) - mean * mean, 0.f);
    sstat[tid * 2] = mean;
    sstat[tid * 2 + 1] = rsqrtf(var + 1e-5f);
  }
  if (tid < 192) {
    const int e = tid, ct = e >> 5, isup = (e >> 4) & 1;
    const int gi = isup * 86 + (f0 >> 4) + (ct < cnt ? ct : 0);
    const float* cp = (const float*)(ws + WS_CS) + (size_t)gi * 128 + (e & 15);
    float a = 0.f;
#pragma unroll
    for (int i = 0; i < 8; ++i) a += cp[i * 16];
    scs[e] = a;
  }

  const int r32 = lane & 31, h = lane >> 5, g = lane >> 4, q4 = (lane & 15) >> 2, p4 = lane & 3;
  const int wm = w >> 1, wn = w & 1;
  if (loader) {
#pragma unroll
    for (int c = 0; c < 8; ++c) {
      if (c < 7) asm volatile("s_waitcnt vmcnt(10)" ::: "memory");
      else       asm volatile("s_waitcnt vmcnt(0)" ::: "memory");
      __builtin_amdgcn_s_barrier();
      if (c + 2 < 8) K4_DMA(c + 2);
    }
    return;
  }
  f32x16 acc[2][3] = {};
#pragma unroll
  for (int c = 0; c < 8; ++c) {
    asm volatile("s_waitcnt lgkmcnt(0)" ::: "memory");
    __builtin_amdgcn_s_barrier();
    if (c == 0) TS(3, 1);
    const char* sA = smem + (c % 3) * K4_ST;
    const char* sB = sA + 16384;
#pragma unroll
    for (int s = 0; s < 4; ++s) {
      bf16x8 af[2], bf[3];
#pragma unroll
      for (int i = 0; i < 2; ++i) {
        const int arow = wm * 64 + i * 32 + r32;
        af[i] = *(const bf16x8*)(sA + arow * 128 + (((2 * s + h) ^ ((arow >> 1) & 7)) << 4));
      }
#pragma unroll
      for (int j = 0; j < 3; ++j) bf[j] = *(const bf16x8*)(sB + ((wn * 3 + j) * 4 + s) * 1024 + lane * 16);
#pragma unroll
      for (int i = 0; i < 2; ++i)
#pragma unroll
        for (int j = 0; j < 3; ++j)
          acc[i][j] = __builtin_amdgcn_mfma_f32_32x32x16_bf16(bf[j], af[i], acc[i][j], 0, 0, 0);
    }
  }
  TS(3, 2);
#pragma unroll
  for (int i2 = 0; i2 < 2; ++i2) {
    const int arow = wm * 64 + i2 * 32 + r32;
    const float mean = sstat[arow * 2], rstd = sstat[arow * 2 + 1];
#pragma unroll
    for (int j = 0; j < 3; ++j) {
      if (wn * 3 + j < cnt) {
        const int fc = f0 + (wn * 3 + j) * 16 + 4 * h;
        const float* cp = scs + (wn * 3 + j) * 32 + 4 * h;
        const f32x4 g0 = *(const f32x4*)(cp), g1 = *(const f32x4*)(cp + 8);
        const f32x4 u0 = *(const f32x4*)(cp + 16), u1 = *(const f32x4*)(cp + 24);
        short* go = (short*)(ws + WS_G) + (size_t)(m0 + arow) * FF + fc;
        float r[8];
#pragma unroll
        for (int i = 0; i < 8; ++i) {
          const float gt = (acc[i2][j][i] - mean * (i < 4 ? g0[i & 3] : g1[i & 3])) * rstd;
          const float up = (acc[i2][j][i + 8] - mean * (i < 4 ? u0[i & 3] : u1[i & 3])) * rstd;
          r[i] = gt * __builtin_amdgcn_rcpf(1.f + __expf(-gt)) * up;
        }
        u32x2 a, c;
        a[0] = cvtpk(r[0], r[1]); a[1] = cvtpk(r[2], r[3]);
        c[0] = cvtpk(r[4], r[5]); c[1] = cvtpk(r[6], r[7]);
        *(u32x4*)(go + 4 * h) = widen16(a, c);
      }
    }
  }
  TS(3, 3);
#undef K4_DMA
}

extern "C" void kernel_launch(void* const* d_in, const int* in_sizes, int n_in, void* d_out, int out_size,
                              void* d_ws, size_t ws_size, hipStream_t stream) {
  const short* x = (const short*)d_in[0];
  const short* wq = (const short*)d_in[1];
  const short* wk = (const short*)d_in[2];
  const short* wv = (const short*)d_in[3];
  const short* wo = (const short*)d_in[4];
  const short* w1 = (const short*)d_in[6];
  const short* w3 = (const short*)d_in[7];
  const short* w2 = (const short*)d_in[8];
  char* ws = (char*)d_ws;
  (void)hipFuncSetAttribute((const void*)k1_ln_qkv, hipFuncAttributeMaxDynamicSharedMemorySize, K1_LDS);
  (void)hipFuncSetAttribute((const void*)k2_attn, hipFuncAttributeMaxDynamicSharedMemorySize, ATT_LDS);
  (void)hipFuncSetAttribute((const void*)k_gemm64<DM, true>, hipFuncAttributeMaxDynamicSharedMemorySize, KG_LDS);
  (void)hipFuncSetAttribute((const void*)k4_ffn1, hipFuncAttributeMaxDynamicSharedMemorySize, K4_LDS);
  (void)hipFuncSetAttribute((const void*)k_gemm64<FF, false>, hipFuncAttributeMaxDynamicSharedMemorySize, KG_LDS);
  k1_ln_qkv<<<256, 512, K1_LDS, stream>>>(x, wq, wk, wv, ws);
  k2_attn<<<256, 1024, ATT_LDS, stream>>>(w1, w3, wo, w2, ws);
  k_gemm64<DM, true><<<256, 512, KG_LDS, stream>>>((const short*)(ws + WS_O), ws + WS_WOF, x, (short*)(ws + WS_H), ws);
  k4_ffn1<<<256, 512, K4_LDS, stream>>>(ws);
  k_gemm64<FF, false><<<256, 512, KG_LDS, stream>>>((const short*)(ws + WS_G), ws + WS_W2F, (const short*)(ws + WS_H), (short*)d_out, ws);
}
```

```cpp
#include <hip/hip_runtime.h>
#include <hip/hip_bf16.h>

typedef __attribute__((ext_vector_type(8))) short bf16x8;
typedef __attribute__((ext_vector_type(4))) short s16x4;
typedef __attribute__((ext_vector_type(16))) float f32x16;
typedef __attribute__((ext_vector_type(4))) float f32x4;
typedef __attribute__((ext_vector_type(2))) float f32x2;
typedef __attribute__((ext_vector_type(4))) unsigned u32x4;
typedef __attribute__((ext_vector_type(2))) unsigned u32x2;
typedef __attribute__((ext_vector_type(2))) __bf16 bf16x2_t;
typedef __attribute__((address_space(3))) s16x4 lds_s16x4;

#define DI __device__ __forceinline__

constexpr int S_LEN = 2048, DM = 512, NH = 32, DH = 16, FF = 1376;
constexpr float QSCALE = 0.25f * 1.4426950408889634f;

constexpr size_t WS_Q = 0;
constexpr size_t WS_KF = WS_Q + (size_t)S_LEN * DM * 2;
constexpr size_t WS_VF = WS_KF + (size_t)S_LEN * DH * 2;
constexpr size_t WS_O = WS_VF + (size_t)S_LEN * DH * 2;
constexpr size_t WS_H = WS_O + (size_t)S_LEN * DM * 2;
constexpr size_t WS_G = WS_H + (size_t)S_LEN * DM * 2;
constexpr size_t WS_KM = WS_G + (size_t)S_LEN * FF * 2;
constexpr size_t WS_ST = WS_KM + 512;
constexpr size_t WS_CS = WS_ST + (size_t)S_LEN * 32 * 4;
constexpr size_t WS_WOF = WS_CS + 172 * 128 * 4;
constexpr size_t WS_W13F = WS_WOF + 512 * 1024;
constexpr size_t WS_W2F = WS_W13F + 2752 * 1024;

DI unsigned cvtpk(float lo, float hi) {
  const f32x2 v = {lo, hi};
  return __builtin_bit_cast(unsigned, __builtin_convertvector(v, bf16x2_t));
}
DI float bf2f(short v) { return __uint_as_float(((unsigned)(unsigned short)v) << 16); }
DI float bflo(unsigned u) { return __uint_as_float(u << 16); }
DI float bfhi(unsigned u) { return __uint_as_float(u & 0xffff0000u); }
DI s16x4 tr_read(const char* p) {
  return __builtin_amdgcn_ds_read_tr16_b64_v4i16((lds_s16x4*)p);
}
DI bf16x8 cat8(s16x4 a, s16x4 b) { return __builtin_shufflevector(a, b, 0, 1, 2, 3, 4, 5, 6, 7); }
DI void stat2(unsigned u, float& s, float& q) {
  const float a = bflo(u), b = bfhi(u);
  s += a + b;
  q = fmaf(a, a, fmaf(b, b, q));
}
DI void stat8(const bf16x8& v, float& s, float& q) {
  const u32x4 u = __builtin_bit_cast(u32x4, v);
#pragma unroll
  for (int j = 0; j < 4; ++j) stat2(u[j], s, q);
}
typedef __attribute__((address_space(3))) void lvoid_t;
DI void glds16(const void* gsrc, unsigned lds_dst) {
  unsigned keep;
  asm volatile("s_mov_b32 %0, m0\n\ts_mov_b32 m0, %2\n\ts_nop 0\n\tglobal_load_lds_dwordx4 %1, off\n\ts_mov_b32 m0, %0"
               : "=&s"(keep) : "v"(gsrc), "s"(lds_dst) : "memory");
}
DI void st16_wt(void* p, u32x4 v) {
  asm volatile("global_store_dwordx4 %0, %1, off sc1\n\ts_nop 1" :: "v"(p), "v"(v) : "memory");
}
DI u32x4 widen16(u32x2 a, u32x2 b) {
  const auto r0 = __builtin_amdgcn_permlane32_swap(a[0], b[0], false, false);
  const auto r1 = __builtin_amdgcn_permlane32_swap(a[1], b[1], false, false);
  return u32x4{r0[0], r1[0], r0[1], r1[1]};
}


constexpr int K1_ST = 36864;
constexpr int K1_LDS = 4 * K1_ST + 1024;
__global__ __launch_bounds__(512) void k1_ln_qkv(const short* __restrict__ x, const short* __restrict__ wq,
                                                 const short* __restrict__ wk, const short* __restrict__ wv,
                                                 char* __restrict__ ws) {
  extern __shared__ __attribute__((aligned(16))) char smem[];
  float* sstat = (float*)(smem + 4 * K1_ST);
  const int tid = threadIdx.x, lane = tid & 63, w = __builtin_amdgcn_readfirstlane(tid >> 6);
  const int b = blockIdx.x, xcd = b & 7, idx = b >> 3;
  const int mt = xcd * 4 + (idx & 3), nt = idx >> 2;
  const int m0 = mt * 64, n0 = nt * 64;
  const int ar = tid >> 4, ac = tid & 15;
  const short* wkv = (nt & 1) ? wv : wk;
  {
    const unsigned lbase = (unsigned)(size_t)(lvoid_t*)smem;
    const short* asrc[2];
    const short* bsrc[2];
#pragma unroll
    for (int i = 0; i < 2; ++i) {
      const int row = (w * 2 + i) * 4 + (lane >> 4);
      asrc[i] = x + (size_t)(m0 + row) * DM + (((lane & 15) ^ (row & 15)) << 3);
      const int k = (w * 2 + i) * 8 + (lane >> 3);
      bsrc[i] = wq + (size_t)k * DM + n0 + (((lane & 7) ^ (((k >> 1) & 1) << 2)) << 3);
    }
    const short* csrc = wkv + ((w & 3) * 64 + lane) * 8;
#pragma unroll
    for (int c = 0; c < 4; ++c) {
      const unsigned st_ = lbase + c * K1_ST;
#pragma unroll
      for (int i = 0; i < 2; ++i) {
        glds16(asrc[i] + c * 128, st_ + (w * 2 + i) * 1024);
        glds16(bsrc[i] + (size_t)c * 128 * DM, st_ + 16384 + (w * 2 + i) * 1024);
      }
      glds16(csrc + c * 2048, st_ + 32768 + (w & 3) * 1024);
    }
  }
  float ssum[2] = {0.f, 0.f}, ssq[2] = {0.f, 0.f};

  const int r32 = lane & 31, h = lane >> 5, g = lane >> 4, q4 = (lane & 15) >> 2, p4 = lane & 3;
  const int wq_ = w & 3, kp = w >> 2;
  const int wm = wq_ >> 1, wn = wq_ & 1;
  const int arow = wm * 32 + r32;
  const int nb = (wn * 32 + (g & 1) * 16 + 4 * p4) * 2;
  const int rb = (nt >> 1) * 16;
  bf16x8 ones;
#pragma unroll
  for (int j = 0; j < 8; ++j) ones[j] = (short)0x3F80;
  f32x16 acc = {}, cs = {};
  f32x4 c4 = {}, cs4 = {};
#pragma unroll
  for (int c = 0; c < 4; ++c) {
    if (c == 0)      asm volatile("s_waitcnt vmcnt(15)" ::: "memory");
    else if (c == 1) asm volatile("s_waitcnt vmcnt(10)" ::: "memory");
    else if (c == 2) asm volatile("s_waitcnt vmcnt(5)" ::: "memory");
    else             asm volatile("s_waitcnt vmcnt(0)" ::: "memory");
    __builtin_amdgcn_s_barrier();
    const char* sA = smem + c * K1_ST;
    const char* sB = sA + 16384;
    bf16x8 af[4], bf[4];
#pragma unroll
    for (int ss = 0; ss < 4; ++ss) {
      const int s = 2 * ss + kp;
      af[ss] = *(const bf16x8*)(sA + arow * 256 + (((2 * s + h) ^ (arow & 15)) << 4));
      const int k0 = 16 * s + 8 * h + q4;
      bf[ss] = cat8(tr_read(sB + k0 * 128 + (nb ^ (((k0 >> 1) & 1) << 6))),
                    tr_read(sB + (k0 + 4) * 128 + (nb ^ ((((k0 + 4) >> 1) & 1) << 6))));
    }
#pragma unroll
    for (int i = 0; i < 2; ++i) {
      const int row = i * 32 + ar;
      stat8(*(const bf16x8*)(sA + row * 256 + ((ac ^ (row & 15)) << 4)), ssum[i], ssq[i]);
    }
#pragma unroll
    for (int ss = 0; ss < 4; ++ss) {
      acc = __builtin_amdgcn_mfma_f32_32x32x16_bf16(bf[ss], af[ss], acc, 0, 0, 0);
      cs = __builtin_amdgcn_mfma_f32_32x32x16_bf16(bf[ss], ones, cs, 0, 0, 0);
    }
    if (w == 5) {
      const char* sC = sA + 32768;
      const int krow = rb + (lane & 15);
#pragma unroll
      for (int s = 0; s < 4; ++s) {
        const bf16x8 a16 = *(const bf16x8*)(sA + krow * 256 + (((4 * s + g) ^ (krow & 15)) << 4));
        const int k0 = 32 * s + 8 * g + q4;
        const bf16x8 b16 = cat8(tr_read(sC + k0 * 32 + 8 * p4), tr_read(sC + (k0 + 4) * 32 + 8 * p4));
        if (nt & 1) {
          c4 = __builtin_amdgcn_mfma_f32_16x16x32_bf16(a16, b16, c4, 0, 0, 0);
          cs4 = __builtin_amdgcn_mfma_f32_16x16x32_bf16(ones, b16, cs4, 0, 0, 0);
        } else {
          c4 = __builtin_amdgcn_mfma_f32_16x16x32_bf16(b16, a16, c4, 0, 0, 0);
          cs4 = __builtin_amdgcn_mfma_f32_16x16x32_bf16(b16, ones, cs4, 0, 0, 0);
        }
      }
    }
  }
#pragma unroll
  for (int i = 0; i < 2; ++i) {
    float s_ = ssum[i], q_ = ssq[i];
#pragma unroll
    for (int o = 1; o < 16; o <<= 1) { s_ += __shfl_xor(s_, o); q_ += __shfl_xor(q_, o); }
    if (ac == 0) {
      const float mean = s_ * (1.f / DM);
      const float var = fmaxf(q_ * (1.f / DM) - mean * mean, 0.f);
      sstat[(i * 32 + ar) * 2] = mean;
      sstat[(i * 32 + ar) * 2 + 1] = rsqrtf(var + 1e-5f);
    }
  }
  __syncthreads();
  const float mean = sstat[arow * 2], rstd = sstat[arow * 2 + 1];
#pragma unroll
  for (int i = 0; i < 16; ++i) acc[i] = fmaf(-mean, cs[i], acc[i]);
  float* red = (float*)smem + wq_ * 1024 + lane;
  if (kp == 1) {
#pragma unroll
    for (int i = 0; i < 16; ++i) red[i * 64] = acc[i];
  }
  if (w == 5) {
    float r[4];
    u32x2 o;
    if (nt & 1) {
#pragma unroll
      for (int i = 0; i < 4; ++i) {
        const int rr = rb + 4 * g + i;
        r[i] = (c4[i] - sstat[rr * 2] * cs4[i]) * sstat[rr * 2 + 1];
      }
      o[0] = cvtpk(r[0], r[1]); o[1] = cvtpk(r[2], r[3]);
      const int key0 = m0 + rb;
      const int t = key0 >> 5, s = (key0 >> 4) & 1;
      short* vf = (short*)(ws + WS_VF) + (size_t)t * 512 + s * 256 + (g & 1) * 128 + (lane & 15) * 8 + (g >> 1) * 4;
      *(u32x2*)vf = o;
    } else {
      const int rr = rb + (lane & 15);
      const float mn = sstat[rr * 2], rs = sstat[rr * 2 + 1];
#pragma unroll
      for (int i = 0; i < 4; ++i) r[i] = (c4[i] - mn * cs4[i]) * rs;
      o[0] = cvtpk(r[0], r[1]); o[1] = cvtpk(r[2], r[3]);
      const int key = m0 + rr;
      const int t = key >> 5;
      short* kf = (short*)(ws + WS_KF) + (size_t)t * 512 + (g >> 1) * 256 + (key & 31) * 8 + (g & 1) * 4;
      *(u32x2*)kf = o;
      const float k0 = bflo(o[0]), k1 = bfhi(o[0]), k2 = bflo(o[1]), k3 = bfhi(o[1]);
      float a = k0 * k0 + k1 * k1 + k2 * k2 + k3 * k3;
      a += __shfl_xor(a, 16);
      a += __shfl_xor(a, 32);
#pragma unroll
      for (int of = 8; of > 0; of >>= 1) a = fmaxf(a, __shfl_xor(a, of));
      if (lane == 0) ((float*)(ws + WS_KM))[mt * 4 + (nt >> 1)] = a;
    }
  }
  __syncthreads();
  if (kp == 1) return;
  const float sc = rstd * QSCALE;
  short* qo = (short*)(ws + WS_Q) + (size_t)(m0 + arow) * DM + n0 + wn * 32 + 4 * h;
#pragma unroll
  for (int gp = 0; gp < 2; ++gp) {
    u32x2 o[2];
#pragma unroll
    for (int e = 0; e < 2; ++e) {
      const int gg = 2 * gp + e;
      o[e][0] = cvtpk((acc[4 * gg] + red[(4 * gg) * 64]) * sc, (acc[4 * gg + 1] + red[(4 * gg + 1) * 64]) * sc);
      o[e][1] = cvtpk((acc[4 * gg + 2] + red[(4 * gg + 2) * 64]) * sc, (acc[4 * gg + 3] + red[(4 * gg + 3) * 64]) * sc);
    }
    *(u32x4*)(qo + 4 * h + 16 * gp) = widen16(o[0], o[1]);
  }
}

constexpr int TS = 2080;
constexpr int ATT_RED = 64 * TS;
constexpr int ATT_LDS = ATT_RED + 8 * 64 * 9 * 4 + 8 * 1024;

__global__ __launch_bounds__(1024) void k2_attn(const short* __restrict__ w1, const short* __restrict__ w3,
                                                const short* __restrict__ wo, const short* __restrict__ w2,
                                                char* __restrict__ ws) {
  extern __shared__ __attribute__((aligned(16))) char smem[];
  const int tid = threadIdx.x, lane = tid & 63, w = tid >> 6;
  const int r32 = lane & 31, h = lane >> 5;
  float* scomb = (float*)(smem + ATT_RED);
  const int wr = w & 7, kh = w >> 3;
  const int pblk = (blockIdx.x & 7) * 32 + (blockIdx.x >> 3);
  const size_t row = (size_t)pblk * 256 + wr * 32 + r32;
  const int tl0 = (w < 8) ? w : 24 + w;
  bf16x8 kv[8];
  {
    const bf16x8* srck = (const bf16x8*)(ws + WS_KF) + tl0 * 64 + lane;
    const bf16x8* srcv = (const bf16x8*)(ws + WS_VF) + tl0 * 64 + lane;
#pragma unroll
    for (int j = 0; j < 4; ++j) { kv[2 * j] = srck[j * 512]; kv[2 * j + 1] = srcv[j * 512]; }
  }
  const bf16x8 qf = *(const bf16x8*)((const short*)(ws + WS_Q) + row * 16 + h * 8);
  float kn2 = fmaxf(((const float*)(ws + WS_KM))[lane], ((const float*)(ws + WS_KM))[64 + lane]);
  bf16x8 csv0, csv1, rl;
  if (tid < 512) *(unsigned*)(smem + (tid >> 3) * TS + 2048 + (tid & 7) * 4) = (tid & 7) < 4 ? 0u : 0x3F803F80u;
  char* sdst = smem + tl0 * TS + lane * 16;
#pragma unroll
  for (int j = 0; j < 4; ++j) {
    *(bf16x8*)(sdst + j * 8 * TS) = kv[2 * j];
    *(bf16x8*)(sdst + j * 8 * TS + 1024) = kv[2 * j + 1];
  }
#pragma unroll
  for (int o = 32; o > 0; o >>= 1) kn2 = fmaxf(kn2, __shfl_xor(kn2, o));
  float qn2 = 0.f;
#pragma unroll
  for (int j = 0; j < 8; ++j) { const float f = bf2f(qf[j]); qn2 += f * f; }
  qn2 += __shfl_xor(qn2, 32);
  const float mref = sqrtf(qn2 * kn2) * 1.001f;

  const bool vlane = r32 < 16;
  const int kbase = lane * 16;
  const int vbase0 = vlane ? (1024 + h * 256 + r32 * 16) : (2048 + (r32 == 16 ? 16 : 0));
  const int vbase1 = vlane ? vbase0 + 512 : vbase0;
  f32x16 o = {};
  {
    f32x16 negm;
#pragma unroll
    for (int i = 0; i < 16; ++i) negm[i] = -mref;
    const char* kp = smem + kbase + kh * 32 * TS;
    const char* vp0 = smem + vbase0 + kh * 32 * TS;
    const char* vp1 = smem + vbase1 + kh * 32 * TS;
#pragma unroll
    for (int j = 0; j < 4; ++j) {
      if (j == 0) __syncthreads();
      if (j == 0) {
  {
    const int gi = blockIdx.x < 172 ? blockIdx.x : 0, t2 = tid & 511;
    const short* wsrc = ((gi < 86) ? (w1 + gi * 16) : (w3 + (gi - 86) * 16)) + (size_t)(t2 >> 1) * FF + (t2 & 1) * 8;
    csv0 = *(const bf16x8*)(wsrc);
    csv1 = *(const bf16x8*)(wsrc + (size_t)256 * FF);
  }
      }
      if (j >= 1 && j <= 2) {
        const int u = blockIdx.x * 16 + w + (j - 1) * 4096;
        if (u < 4640) {
          char* scr = (char*)scomb + (kh ? wr * 2304 : 18432 + wr * 1024);
          *(bf16x8*)(scr + (lane >> 2) * 64 + (lane & 3) * 16) = rl;
          const int g4 = lane >> 4;
          const char* rp = scr + (8 * (g4 >> 1) + ((lane & 15) >> 2)) * 64 + ((g4 & 1) * 16 + 4 * (lane & 3)) * 2;
          char* dst = ws + WS_WOF + (size_t)u * 1024;
          st16_wt(dst + lane * 16, __builtin_bit_cast(u32x4, cat8(tr_read(rp), tr_read(rp + 256))));
        }
      }
      if (j <= 1) {
        int u = blockIdx.x * 16 + w + j * 4096;
        u = u < 4640 ? u : 0;
        const int kk = lane >> 2, pc = lane & 3;
        const short* src;
        if (u < 512) {
          src = wo + (size_t)(16 * (u & 31) + kk) * DM + 32 * (u >> 5) + pc * 8;
        } else if (u < 3264) {
          const int v = u - 512;
          src = (pc < 2 ? w1 : w3) + (size_t)(16 * (v & 31) + kk) * FF + (v >> 5) * 16 + (pc & 1) * 8;
        } else {
          const int v = u - 3264;
          src = w2 + (size_t)(16 * (v % 86) + kk) * DM + 32 * (v / 86) + pc * 8;
        }
        rl = *(const bf16x8*)src;
      }
#pragma unroll
      for (int t = j * 8; t < j * 8 + 8; ++t) {
        const bf16x8 kf = *(const bf16x8*)(kp + t * TS);
        const bf16x8 vf0 = *(const bf16x8*)(vp0 + t * TS);
        const bf16x8 vf1 = *(const bf16x8*)(vp1 + t * TS);
        const f32x16 s = __builtin_amdgcn_mfma_f32_32x32x16_bf16(kf, qf, negm, 0, 0, 0);
        u32x4 p0, p1;
#pragma unroll
        for (int i = 0; i < 4; ++i) {
          p0[i] = cvtpk(__builtin_amdgcn_exp2f(s[2 * i]), __builtin_amdgcn_exp2f(s[2 * i + 1]));
          p1[i] = cvtpk(__builtin_amdgcn_exp2f(s[8 + 2 * i]), __builtin_amdgcn_exp2f(s[8 + 2 * i + 1]));
        }
        o = __builtin_amdgcn_mfma_f32_32x32x16_bf16(vf0, __builtin_bit_cast(bf16x8, p0), o, 0, 0, 0);
        o = __builtin_amdgcn_mfma_f32_32x32x16_bf16(vf1, __builtin_bit_cast(bf16x8, p1), o, 0, 0, 0);
      }
    }
  }
  float* cb = scomb + wr * (64 * 9) + lane * 9;
  if (kh == 1) {
#pragma unroll
    for (int i = 0; i < 9; ++i) cb[i] = o[i];
  }
  __syncthreads();
  if (kh == 1) {
    if (blockIdx.x < 172) {
      float cf[8];
#pragma unroll
      for (int j = 0; j < 8; ++j) {
        cf[j] = bf2f(csv0[j]) + bf2f(csv1[j]);
#pragma unroll
        for (int of = 2; of < 64; of <<= 1) cf[j] += __shfl_xor(cf[j], of);
      }
      if (lane < 2) {
        float* dst = (float*)(ws + WS_CS) + (size_t)(blockIdx.x * 8 + wr) * 16 + lane * 8;
        *(f32x4*)dst = f32x4{cf[0], cf[1], cf[2], cf[3]};
        *(f32x4*)(dst + 4) = f32x4{cf[4], cf[5], cf[6], cf[7]};
      }
    }
    return;
  }
#pragma unroll
  for (int i = 0; i < 9; ++i) o[i] += cb[i];
  float l = __shfl(o[8], r32);
  if (__any(l < 1e-18f)) {
    f32x16 negm = {};
    o = f32x16{};
#pragma unroll 1
    for (int t = 0; t < 64; ++t) {
      const bf16x8 kf = *(const bf16x8*)(smem + kbase + t * TS);
      const bf16x8 vf0 = *(const bf16x8*)(smem + vbase0 + t * TS);
      const bf16x8 vf1 = *(const bf16x8*)(smem + vbase1 + t * TS);
      f32x16 s = __builtin_amdgcn_mfma_f32_32x32x16_bf16(kf, qf, negm, 0, 0, 0);
      float mx = s[0];
#pragma unroll
      for (int i = 1; i < 16; ++i) mx = fmaxf(mx, s[i]);
      float d = fmaxf(mx, __shfl_xor(mx, 32));
      if (t != 0) d = fmaxf(d, 0.f);
      const float sc = (t != 0) ? __builtin_amdgcn_exp2f(-d) : 1.f;
#pragma unroll
      for (int i = 0; i < 16; ++i) { s[i] -= d; negm[i] -= d; o[i] *= sc; }
      u32x4 p0, p1;
#pragma unroll
      for (int i = 0; i < 4; ++i) {
        p0[i] = cvtpk(__builtin_amdgcn_exp2f(s[2 * i]), __builtin_amdgcn_exp2f(s[2 * i + 1]));
        p1[i] = cvtpk(__builtin_amdgcn_exp2f(s[8 + 2 * i]), __builtin_amdgcn_exp2f(s[8 + 2 * i + 1]));
      }
      o = __builtin_amdgcn_mfma_f32_32x32x16_bf16(vf0, __builtin_bit_cast(bf16x8, p0), o, 0, 0, 0);
      o = __builtin_amdgcn_mfma_f32_32x32x16_bf16(vf1, __builtin_bit_cast(bf16x8, p1), o, 0, 0, 0);
    }
    l = __shfl(o[8], r32);
  }
  const float inv = 1.f / l;
  short* op = (short*)(ws + WS_O) + row * 16 + 4 * h;
  u32x2 a, c;
  a[0] = cvtpk(o[0] * inv, o[1] * inv); a[1] = cvtpk(o[2] * inv, o[3] * inv);
  c[0] = cvtpk(o[4] * inv, o[5] * inv); c[1] = cvtpk(o[6] * inv, o[7] * inv);
  *(u32x4*)(op + 4 * h) = widen16(a, c);
}

constexpr int KG_LDS = 4 * 32768;
template <int KTOT, bool STATS>
__global__ __launch_bounds__(512) void k_gemm64(const short* __restrict__ A, const char* __restrict__ BF,
                                                const short* __restrict__ resid, short* __restrict__ out,
                                                char* __restrict__ ws) {
  extern __shared__ __attribute__((aligned(16))) char smem[];
  constexpr int NCH = (KTOT + 127) / 128;
  constexpr int KLAST = KTOT - (NCH - 1) * 128;
  constexpr int NKS = KTOT / 16;
  constexpr int NST = 4, DD = 3;
  const int tid = threadIdx.x, lane = tid & 63, w = __builtin_amdgcn_readfirstlane(tid >> 6);
  const int b = blockIdx.x, xcd = b & 7, idx = b >> 3;
  const int mt = xcd * 4 + (idx & 3), nt = idx >> 2;
  const int m0 = mt * 64, n0 = nt * 64;
  const short* asrc[2];
  const char* bsrc[2];
  int aco[2];
#pragma unroll
  for (int i = 0; i < 2; ++i) {
    const int row = (w * 2 + i) * 4 + (lane >> 4);
    aco[i] = ((lane & 15) ^ (row & 15)) << 3;
    asrc[i] = A + (size_t)(m0 + row) * KTOT;
    const int e = w * 2 + i;
    bsrc[i] = BF + (size_t)((nt * 2 + (e >> 3)) * NKS + (e & 7)) * 1024 + lane * 16;
  }
  const unsigned lbase = (unsigned)(size_t)(lvoid_t*)smem;
#define KG_DMA(c) do { const unsigned st_ = lbase + ((c) % NST) * 32768; _Pragma("unroll") for (int i = 0; i < 2; ++i) { \
      const int ka = (((c) < NCH - 1) || (aco[i] < KLAST)) ? (c) * 128 + aco[i] : 0; \
      glds16(asrc[i] + ka, st_ + (w * 2 + i) * 1024); \
      const int kb = (((c) < NCH - 1) || (((w * 2 + i) & 7) * 16 < KLAST)) ? (c) * 8192 : 0; \
      glds16(bsrc[i] + kb, st_ + 16384 + (w * 2 + i) * 1024); } } while (0)
  const int r32 = lane & 31, h = lane >> 5;
  const int wq_ = w & 3, kp = w >> 2;
  const int wm = wq_ >> 1, wn = wq_ & 1;
  const int arow = wm * 32 + r32;
  const size_t off = (size_t)(m0 + arow) * DM + n0 + wn * 32 + 4 * h;
  s16x4 xr[4];
#pragma unroll
  for (int gg = 0; gg < 4; ++gg) xr[gg] = *(const s16x4*)(resid + off + 8 * gg);
  asm volatile("" ::: "memory");
  KG_DMA(0); KG_DMA(1); KG_DMA(2);
  f32x16 acc = {};
#pragma unroll
  for (int c = 0; c < NCH; ++c) {
    if (c < NCH - 2)       asm volatile("s_waitcnt vmcnt(8)" ::: "memory");
    else if (c == NCH - 2) asm volatile("s_waitcnt vmcnt(4)" ::: "memory");
    else                   asm volatile("s_waitcnt vmcnt(0)" ::: "memory");
    asm volatile("s_waitcnt lgkmcnt(0)" ::: "memory");
    __builtin_amdgcn_s_barrier();
    if (c + DD < NCH) KG_DMA(c + DD);
    const char* sA = smem + (c % NST) * 32768;
    const char* sB = sA + 16384;
#pragma unroll
    for (int ss = 0; ss < 4; ++ss) {
      const int s = 2 * ss + kp;
      if (c < NCH - 1 || 16 * (2 * ss) < KLAST) {
        const bf16x8 af = *(const bf16x8*)(sA + arow * 256 + (((2 * s + h) ^ (arow & 15)) << 4));
        const bf16x8 bf = *(const bf16x8*)(sB + (wn * 8 + s) * 1024 + lane * 16);
        acc = __builtin_amdgcn_mfma_f32_32x32x16_bf16(bf, af, acc, 0, 0, 0);
      }
    }
  }
  float* red = (float*)(smem + (NCH % NST) * 32768) + wq_ * 1024 + lane;
  if (kp == 1) {
#pragma unroll
    for (int i = 0; i < 16; ++i) red[i * 64] = acc[i];
  }
  __syncthreads();
  if (kp == 1) return;
  float ssum = 0.f, ssq = 0.f;
#pragma unroll
  for (int gp = 0; gp < 2; ++gp) {
    u32x2 o[2];
#pragma unroll
    for (int e = 0; e < 2; ++e) {
      const int gg = 2 * gp + e;
      o[e][0] = cvtpk(acc[4 * gg] + red[(4 * gg) * 64] + bf2f(xr[gg][0]), acc[4 * gg + 1] + red[(4 * gg + 1) * 64] + bf2f(xr[gg][1]));
      o[e][1] = cvtpk(acc[4 * gg + 2] + red[(4 * gg + 2) * 64] + bf2f(xr[gg][2]), acc[4 * gg + 3] + red[(4 * gg + 3) * 64] + bf2f(xr[gg][3]));
      if (STATS) {
        stat2(o[e][0], ssum, ssq);
        stat2(o[e][1], ssum, ssq);
      }
    }
    *(u32x4*)(out + off + 4 * h + 16 * gp) = widen16(o[0], o[1]);
  }
  if (STATS) {
    ssum += __shfl_xor(ssum, 32);
    ssq += __shfl_xor(ssq, 32);
    if (h == 0) {
      f32x2 st = {ssum, ssq};
      *(f32x2*)((float*)(ws + WS_ST) + (size_t)(m0 + arow) * 32 + (nt * 2 + wn) * 2) = st;
    }
  }
#undef KG_DMA
}

constexpr int K4_ST = 40960;
constexpr int K4_LDS = 3 * K4_ST + 1024 + 1024;
__global__ __launch_bounds__(512) void k4_ffn1(char* __restrict__ ws) {
  extern __shared__ __attribute__((aligned(16))) char smem[];
  float* sstat = (float*)(smem + 3 * K4_ST);
  float* scs = sstat + 256;
  const int tid = threadIdx.x, lane = tid & 63, wall = __builtin_amdgcn_readfirstlane(tid >> 6);
  const int w = wall & 3;
  const bool loader = wall >= 4;
  const int b = blockIdx.x, xcd = b & 7, idx = b >> 3;
  const int mt = xcd * 2 + (idx & 1), ng = idx >> 1;
  const int m0 = mt * 128;
  const int cnt = ng < 6 ? 6 : 5;
  const int f0 = (ng * 5 + (ng < 6 ? ng : 6)) * 16;
  const short* H = (const short*)(ws + WS_H);
  const short* asrc[4];
  const short* bsrc[6];
#pragma unroll
  for (int i = 0; i < 4; ++i) {
    const int row = (w * 4 + i) * 8 + (lane >> 3);
    asrc[i] = H + (size_t)(m0 + row) * DM + (((lane & 7) ^ ((row >> 1) & 7)) << 3);
  }
#pragma unroll
  for (int i = 0; i < 6; ++i) {
    const int e = w * 6 + i, j = e >> 2, ks = e & 3;
    const int ct = (f0 >> 4) + (j < cnt ? j : 0);
    bsrc[i] = (const short*)(ws + WS_W13F + (size_t)(ct * 32 + ks) * 1024 + lane * 16);
  }
  const unsigned lbase = (unsigned)(size_t)(lvoid_t*)smem;
#define K4_DMA(c) do { const unsigned st_ = lbase + ((c) % 3) * K4_ST; \
    _Pragma("unroll") for (int i = 0; i < 4; ++i) glds16(asrc[i] + (c) * 64, st_ + (w * 4 + i) * 1024); \
    _Pragma("unroll") for (int i = 0; i < 6; ++i) glds16(bsrc[i] + (c) * 2048, st_ + 16384 + (w * 6 + i) * 1024); } while (0)
  if (loader) { K4_DMA(0); K4_DMA(1); }
  if (tid < 128) {
    const f32x4* sp = (const f32x4*)((const float*)(ws + WS_ST) + (size_t)(m0 + tid) * 32);
    float s_ = 0.f, q_ = 0.f;
#pragma unroll
    for (int i = 0; i < 8; ++i) { const f32x4 v = sp[i]; s_ += v[0] + v[2]; q_ += v[1] + v[3]; }
    const float mean = s_ * (1.f / DM);
    const float var = fmaxf(q_ * (1.f / DM) - mean * mean, 0.f);
    sstat[tid * 2] = mean;
    sstat[tid * 2 + 1] = rsqrtf(var + 1e-5f);
  }
  if (tid < 192) {
    const int e = tid, ct = e >> 5, isup = (e >> 4) & 1;
    const int gi = isup * 86 + (f0 >> 4) + (ct < cnt ? ct : 0);
    const float* cp = (const float*)(ws + WS_CS) + (size_t)gi * 128 + (e & 15);
    float a = 0.f;
#pragma unroll
    for (int i = 0; i < 8; ++i) a += cp[i * 16];
    scs[e] = a;
  }

  const int r32 = lane & 31, h = lane >> 5, g = lane >> 4, q4 = (lane & 15) >> 2, p4 = lane & 3;
  const int wm = w >> 1, wn = w & 1;
  if (loader) {
#pragma unroll
    for (int c = 0; c < 8; ++c) {
      if (c < 7) asm volatile("s_waitcnt vmcnt(10)" ::: "memory");
      else       asm volatile("s_waitcnt vmcnt(0)" ::: "memory");
      __builtin_amdgcn_s_barrier();
      if (c + 2 < 8) K4_DMA(c + 2);
    }
    return;
  }
  f32x16 acc[2][3] = {};
#pragma unroll
  for (int c = 0; c < 8; ++c) {
    asm volatile("s_waitcnt lgkmcnt(0)" ::: "memory");
    __builtin_amdgcn_s_barrier();
    const char* sA = smem + (c % 3) * K4_ST;
    const char* sB = sA + 16384;
#pragma unroll
    for (int s = 0; s < 4; ++s) {
      bf16x8 af[2], bf[3];
#pragma unroll
      for (int i = 0; i < 2; ++i) {
        const int arow = wm * 64 + i * 32 + r32;
        af[i] = *(const bf16x8*)(sA + arow * 128 + (((2 * s + h) ^ ((arow >> 1) & 7)) << 4));
      }
#pragma unroll
      for (int j = 0; j < 3; ++j) bf[j] = *(const bf16x8*)(sB + ((wn * 3 + j) * 4 + s) * 1024 + lane * 16);
#pragma unroll
      for (int i = 0; i < 2; ++i)
#pragma unroll
        for (int j = 0; j < 3; ++j)
          acc[i][j] = __builtin_amdgcn_mfma_f32_32x32x16_bf16(bf[j], af[i], acc[i][j], 0, 0, 0);
    }
  }
#pragma unroll
  for (int i2 = 0; i2 < 2; ++i2) {
    const int arow = wm * 64 + i2 * 32 + r32;
    const float mean = sstat[arow * 2], rstd = sstat[arow * 2 + 1];
#pragma unroll
    for (int j = 0; j < 3; ++j) {
      if (wn * 3 + j < cnt) {
        const int fc = f0 + (wn * 3 + j) * 16 + 4 * h;
        const float* cp = scs + (wn * 3 + j) * 32 + 4 * h;
        const f32x4 g0 = *(const f32x4*)(cp), g1 = *(const f32x4*)(cp + 8);
        const f32x4 u0 = *(const f32x4*)(cp + 16), u1 = *(const f32x4*)(cp + 24);
        short* go = (short*)(ws + WS_G) + (size_t)(m0 + arow) * FF + fc;
        float r[8];
#pragma unroll
        for (int i = 0; i < 8; ++i) {
          const float gt = (acc[i2][j][i] - mean * (i < 4 ? g0[i & 3] : g1[i & 3])) * rstd;
          const float up = (acc[i2][j][i + 8] - mean * (i < 4 ? u0[i & 3] : u1[i & 3])) * rstd;
          r[i] = gt * __builtin_amdgcn_rcpf(1.f + __expf(-gt)) * up;
        }
        u32x2 a, c;
        a[0] = cvtpk(r[0], r[1]); a[1] = cvtpk(r[2], r[3]);
        c[0] = cvtpk(r[4], r[5]); c[1] = cvtpk(r[6], r[7]);
        *(u32x4*)(go + 4 * h) = widen16(a, c);
      }
    }
  }
#undef K4_DMA
}

extern "C" void kernel_launch(void* const* d_in, const int* in_sizes, int n_in, void* d_out, int out_size,
                              void* d_ws, size_t ws_size, hipStream_t stream) {
  const short* x = (const short*)d_in[0];
  const short* wq = (const short*)d_in[1];
  const short* wk = (const short*)d_in[2];
  const short* wv = (const short*)d_in[3];
  const short* wo = (const short*)d_in[4];
  const short* w1 = (const short*)d_in[6];
  const short* w3 = (const short*)d_in[7];
  const short* w2 = (const short*)d_in[8];
  char* ws = (char*)d_ws;
  (void)hipFuncSetAttribute((const void*)k1_ln_qkv, hipFuncAttributeMaxDynamicSharedMemorySize, K1_LDS);
  (void)hipFuncSetAttribute((const void*)k2_attn, hipFuncAttributeMaxDynamicSharedMemorySize, ATT_LDS);
  (void)hipFuncSetAttribute((const void*)k_gemm64<DM, true>, hipFuncAttributeMaxDynamicSharedMemorySize, KG_LDS);
  (void)hipFuncSetAttribute((const void*)k4_ffn1, hipFuncAttributeMaxDynamicSharedMemorySize, K4_LDS);
  (void)hipFuncSetAttribute((const void*)k_gemm64<FF, false>, hipFuncAttributeMaxDynamicSharedMemorySize, KG_LDS);
  k1_ln_qkv<<<256, 512, K1_LDS, stream>>>(x, wq, wk, wv, ws);
  k2_attn<<<256, 1024, ATT_LDS, stream>>>(w1, w3, wo, w2, ws);
  k_gemm64<DM, true><<<256, 512, KG_LDS, stream>>>((const short*)(ws + WS_O), ws + WS_WOF, x, (short*)(ws + WS_H), ws);
  k4_ffn1<<<256, 512, K4_LDS, stream>>>(ws);
  k_gemm64<FF, false><<<256, 512, KG_LDS, stream>>>((const short*)(ws + WS_G), ws + WS_W2F, (const short*)(ws + WS_H), (short*)d_out, ws);
}
```
